```python
import torch

def _softmax(x, axis=-1):
    return torch.softmax(x, dim=axis)
def _gelu(x):
    return torch.nn.functional.gelu(x, approximate="tanh")
def _silu(x):
    return torch.nn.functional.silu(x)
def _sigmoid(x):
    return torch.sigmoid(x)
def _layernorm(x, axis=-1, eps=1e-5):
    m = x.mean(dim=axis, keepdim=True)
    v = x.var(dim=axis, keepdim=True, unbiased=False)
    return (x - m) / torch.sqrt(v + eps)
def _scatter_add(tgt, idx, src, axis=0):
    return tgt.clone().index_add_(axis, idx, src)
def _segment_sum(src, seg, n):
    out = torch.zeros((n,) + src.shape[1:], dtype=src.dtype)
    return out.index_add_(0, seg, src)
def _pad(x, pw):
    flat = tuple(v for pair in reversed(pw) for v in pair)
    return torch.nn.functional.pad(x, flat)

kernel_name = 'standalone__gqa_block_cached__near_ridge'

def setup_inputs():

    _w = (lambda a: (lambda t: t.to(torch.bfloat16) if t.is_floating_point() else t)(torch.as_tensor(a)))
    B, Sq, D = 8, 96, 640
    Hq, Hkv, Sk, Dh = 32, 32, 512, 128
    assert Hq % Hkv == 0, (Hq, Hkv)
    return {
        "x": _w((lambda *s: torch.randn(*s, dtype=torch.bfloat16) * 0.8)(B, Sq, D)),
        "k_cache": _w((lambda *s: torch.randn(*s, dtype=torch.bfloat16) * 0.8)(B, Hkv, Sk, Dh)),
        "v_cache": _w((lambda *s: torch.randn(*s, dtype=torch.bfloat16) * 0.05)(B, Hkv, Sk, Dh)),
        "wq": _w((lambda *s: torch.randn(*s, dtype=torch.bfloat16) * 0.8)(D, Hq * Dh) * (D ** -0.5)),
        "wo": _w((lambda *s: torch.randn(*s, dtype=torch.bfloat16) * 0.05)(Hq * Dh, D) * ((Hq * Dh) ** -0.5)),
    }

def reference(x, k_cache, v_cache, wq, wo):
    B, Sq, D = x.shape
    Hq, Dh = 32, 128
    Hkv = k_cache.shape[1]
    rep = Hq // Hkv
    q = (lambda x, *d: x.permute(*d))((x @ wq).reshape(B, Sq, Hq, Dh), 0, 2, 1, 3)
    k = (lambda x, n, axis: torch.repeat_interleave(x, n, dim=axis))(k_cache, rep, axis=1)
    v = (lambda x, n, axis: torch.repeat_interleave(x, n, dim=axis))(v_cache, rep, axis=1)
    scores = torch.einsum("bhqd,bhkd->bhqk", q, k) * (Dh ** -0.5)
    p = _softmax(scores, axis=-1)
    o = torch.einsum("bhqk,bhkd->bhqd", p, v)
    return (lambda x, *d: x.permute(*d))(o, 0, 2, 1, 3).reshape(B, Sq, Hq * Dh) @ wo
```

```cpp
#include <hip/hip_runtime.h>
#include <hip/hip_bf16.h>

#define DI __device__ __forceinline__
typedef __attribute__((ext_vector_type(8))) __bf16 bf16x8;
typedef __attribute__((ext_vector_type(4))) __bf16 bf16x4;
typedef __attribute__((ext_vector_type(2))) __bf16 bf16x2;
typedef __attribute__((ext_vector_type(4))) float f32x4;
typedef __attribute__((ext_vector_type(2))) float f32x2;
typedef __attribute__((ext_vector_type(2))) unsigned u32x2;
typedef __attribute__((address_space(3))) char lds_char;
typedef __attribute__((address_space(3))) bf16x8 lds_bf16x8;
typedef __attribute__((address_space(3))) bf16x4 lds_bf16x4;
typedef __attribute__((address_space(3))) f32x4 lds_f32x4;
typedef __attribute__((address_space(3))) float lds_f32;

constexpr int NB = 8, SQ = 96, DM = 640, NH = 32, SK = 512, DH = 128;
constexpr int HD = NH * DH;
constexpr int MROWS = NB * SQ;
constexpr unsigned SLOT = 32768;
constexpr unsigned LDS_BYTES = 5 * SLOT;

DI void dma16(unsigned m0v, unsigned voff, __amdgpu_buffer_rsrc_t srd, unsigned soff) {
  asm volatile("s_mov_b32 m0, %0\n\ts_nop 0\n\tbuffer_load_dwordx4 %1, %2, %3 offen lds"
               :: "s"(m0v), "v"(voff), "s"(srd), "s"(soff) : "memory");
}
template <int N> DI void wait_bar() {
  asm volatile("s_waitcnt vmcnt(%0) lgkmcnt(0)\n\ts_barrier" :: "i"(N) : "memory");
}
DI void bar_only() { asm volatile("s_waitcnt lgkmcnt(0)\n\ts_barrier" ::: "memory"); }
DI f32x4 mfma16(bf16x8 a, bf16x8 b, f32x4 c) { return __builtin_amdgcn_mfma_f32_16x16x32_bf16(a, b, c, 0, 0, 0); }
DI bf16x8 ld128(lds_char* L, unsigned off) { return *(lds_bf16x8*)(L + off); }
DI bf16x8 ldtr2(lds_char* L, unsigned off0, unsigned off1) {
  bf16x4 a = __builtin_amdgcn_ds_read_tr16_b64_v4bf16((lds_bf16x4*)(L + off0));
  bf16x4 b = __builtin_amdgcn_ds_read_tr16_b64_v4bf16((lds_bf16x4*)(L + off1));
  return __builtin_shufflevector(a, b, 0, 1, 2, 3, 4, 5, 6, 7);
}
DI bf16x4 cvt4(f32x4 v) { return __builtin_convertvector(v, bf16x4); }
DI float xmax16_32(float m) {
  auto r = __builtin_amdgcn_permlane16_swap(__float_as_uint(m), __float_as_uint(m), false, false);
  m = fmaxf(__uint_as_float(r[0]), __uint_as_float(r[1]));
  auto r2 = __builtin_amdgcn_permlane32_swap(__float_as_uint(m), __float_as_uint(m), false, false);
  return fmaxf(__uint_as_float(r2[0]), __uint_as_float(r2[1]));
}
DI float xsum16_32(float m) {
  auto r = __builtin_amdgcn_permlane16_swap(__float_as_uint(m), __float_as_uint(m), false, false);
  m = __uint_as_float(r[0]) + __uint_as_float(r[1]);
  auto r2 = __builtin_amdgcn_permlane32_swap(__float_as_uint(m), __float_as_uint(m), false, false);
  return __uint_as_float(r2[0]) + __uint_as_float(r2[1]);
}

template <int I> struct IC { static constexpr int v = I; };
template <int I0, int I1, class F> DI void static_for(F&& f) {
  if constexpr (I0 < I1) { f(IC<I0>{}); static_for<I0 + 1, I1>(f); }
}

__global__ __launch_bounds__(512, 2)
void attn_kernel(const __bf16* __restrict__ x, const __bf16* __restrict__ kc, const __bf16* __restrict__ vc,
                 const __bf16* __restrict__ wq, __bf16* __restrict__ o_ws) {
  extern __shared__ __attribute__((aligned(16))) char smem[];
  lds_char* L = (lds_char*)smem;
  const unsigned ldsb = (unsigned)(size_t)L;
  const unsigned tid = threadIdx.x, l = tid & 63u;
  unsigned w = __builtin_amdgcn_readfirstlane(tid >> 6);
  asm volatile("s_nop 4" : "+s"(w));
  const unsigned bid = blockIdx.x;
  const unsigned h = (bid & 7u) * 4u + ((bid >> 3) & 3u), b = bid >> 5;

  const __amdgpu_buffer_rsrc_t srdX = __builtin_amdgcn_make_buffer_rsrc((void*)(x + (size_t)b * SQ * DM), (short)0, SQ * DM * 2, 0x00020000);
  const __amdgpu_buffer_rsrc_t srdW = __builtin_amdgcn_make_buffer_rsrc((void*)(wq + h * DH), (short)0, (int)((DM * HD - h * DH) * 2), 0x00020000);
  const __amdgpu_buffer_rsrc_t srdK = __builtin_amdgcn_make_buffer_rsrc((void*)(kc + (size_t)(b * NH + h) * SK * DH), (short)0, SK * DH * 2, 0x00020000);
  const __amdgpu_buffer_rsrc_t srdV = __builtin_amdgcn_make_buffer_rsrc((void*)(vc + (size_t)(b * NH + h) * SK * DH), (short)0, SK * DH * 2, 0x00020000);

  const unsigned g = l >> 4, i16 = l & 15u;
  const unsigned voffX = (8u * w + (l >> 3)) * (DM * 2) + (((l & 7u) ^ ((4u * w + g) & 7u)) << 4);
  const unsigned fW = 2u * (g | (((w >> 1) & 1u) << 2));
  const unsigned voffW = (4u * w + g) * (HD * 2) + ((i16 ^ fW) << 4);
  const unsigned a4 = w & 3u, kvrow = 64u * (w >> 2) + 4u * a4;
  const unsigned voffK = (kvrow + g) * 256u + ((i16 ^ (4u * a4 + g)) << 4);
  const unsigned voffV = (kvrow + g) * 256u + ((i16 ^ (2u * ((4u * a4 + g) & 7u))) << 4);

  auto issue = [&](auto NI) {
    constexpr int n = decltype(NI)::v;
    constexpr unsigned slot = (unsigned)(n % 5) * SLOT;
    if constexpr (n < 10) {
      dma16(ldsb + slot + w * 1024u, voffW, srdW, (unsigned)n * 64u * (HD * 2));
      dma16(ldsb + slot + w * 1024u + 8192u, voffW, srdW, (unsigned)n * 64u * (HD * 2) + 32u * (HD * 2));
      dma16(ldsb + slot + 16384u + w * 1024u, voffX, srdX, (unsigned)n * 128u);
      dma16(ldsb + slot + 16384u + 8192u + w * 1024u, voffX, srdX, (unsigned)n * 128u + 64u * (DM * 2));
    } else if constexpr (n < 14) {
#pragma unroll
      for (int j = 0; j < 4; ++j) dma16(ldsb + slot + kvrow * 256u + j * 4096u, voffK, srdK, (unsigned)(n - 10) * SLOT + j * 4096u);
    } else if constexpr (n >= 15 && n < 19) {
#pragma unroll
      for (int j = 0; j < 4; ++j) dma16(ldsb + slot + kvrow * 256u + j * 4096u, voffV, srdV, (unsigned)(n - 15) * SLOT + j * 4096u);
    }
  };

  static_for<0, 5>([&](auto NI) { issue(NI); });

  const unsigned qg = w >> 2, kq = w & 3u;
  const unsigned q4 = i16 >> 2, p4 = l & 3u;
  f32x4 qacc[2][3];
#pragma unroll
  for (int e = 0; e < 2; ++e)
#pragma unroll
    for (int j = 0; j < 3; ++j) qacc[e][j] = f32x4{0.f, 0.f, 0.f, 0.f};
  {
    const unsigned fWr = 2u * (q4 | ((g & 1u) << 2));
    const unsigned wrd0 = (8u * g + q4) * 256u + (((4u * kq + (p4 >> 1)) ^ fWr) << 4) + 8u * (p4 & 1u);
    const unsigned wrd1 = wrd0 ^ 32u;
    const unsigned xrow = 48u * qg + i16;
    const unsigned xrd0 = 16384u + xrow * 128u + ((g ^ ((xrow >> 1) & 7u)) << 4);
    const unsigned xrd1 = xrd0 ^ 64u;
    static_for<0, 10>([&](auto NI) {
      constexpr int n = decltype(NI)::v;
      constexpr unsigned slot = (unsigned)(n % 5) * SLOT;
      if constexpr (n == 0) wait_bar<16>(); else wait_bar<12>();
      if constexpr (n >= 1) issue(IC<n + 4>{});
#pragma unroll
      for (int ks = 0; ks < 2; ++ks) {
        bf16x8 af[2], bfr[3];
        af[0] = ldtr2(L, slot + wrd0 + ks * 8192u, slot + wrd0 + ks * 8192u + 1024u);
        af[1] = ldtr2(L, slot + wrd1 + ks * 8192u, slot + wrd1 + ks * 8192u + 1024u);
#pragma unroll
        for (int j = 0; j < 3; ++j) bfr[j] = ld128(L, slot + (ks ? xrd1 : xrd0) + j * 2048u);
#pragma unroll
        for (int e = 0; e < 2; ++e)
#pragma unroll
          for (int j = 0; j < 3; ++j) qacc[e][j] = mfma16(af[e], bfr[j], qacc[e][j]);
      }
    });
  }
  constexpr unsigned QIMG = 4 * SLOT, SCR = 4 * SLOT + 24576u;
  bar_only();
#pragma unroll
  for (int e = 0; e < 2; ++e)
#pragma unroll
    for (int j = 0; j < 3; ++j) {
      const unsigned q = 48u * qg + 16u * j + i16, c = 2u * (2u * kq + e) + (g >> 1);
      *(lds_bf16x4*)(L + QIMG + q * 256u + ((c ^ i16) << 4) + 8u * (g & 1u)) = cvt4(qacc[e][j]);
    }
  f32x4 s[4][2][3];
  bf16x8 qf[3][4];
  const unsigned krd = (32u * kq + i16) * 256u + ((g ^ i16) << 4);
  static_for<0, 4>([&](auto TI) {
    constexpr int t = decltype(TI)::v;
    constexpr unsigned slot = (unsigned)t * SLOT;
    if constexpr (t == 0) wait_bar<12>(); else wait_bar<8>();
    if constexpr (t >= 1) issue(IC<14 + t>{});
    if constexpr (t == 0) {
#pragma unroll
      for (int j = 0; j < 3; ++j)
#pragma unroll
        for (int ks = 0; ks < 4; ++ks)
          qf[j][ks] = ld128(L, QIMG + (48u * qg + 16u * j + i16) * 256u + (((4u * ks + g) ^ i16) << 4));
    }
#pragma unroll
    for (int kt = 0; kt < 2; ++kt) {
#pragma unroll
      for (int j = 0; j < 3; ++j) s[t][kt][j] = f32x4{0.f, 0.f, 0.f, 0.f};
#pragma unroll
      for (int ks = 0; ks < 4; ++ks) {
        const bf16x8 kf = ld128(L, slot + (krd ^ (ks << 6)) + kt * 4096u);
#pragma unroll
        for (int j = 0; j < 3; ++j) s[t][kt][j] = mfma16(kf, qf[j][ks], s[t][kt][j]);
      }
    }
  });
  float mx[3];
#pragma unroll
  for (int j = 0; j < 3; ++j) {
    float m = s[0][0][j][0];
#pragma unroll
    for (int t = 0; t < 4; ++t)
#pragma unroll
      for (int kt = 0; kt < 2; ++kt)
#pragma unroll
        for (int r = 0; r < 4; ++r) m = fmaxf(m, s[t][kt][j][r]);
    m = xmax16_32(m);
    if (g == 0) *(lds_f32*)(L + SCR + (w * 48u + 16u * j + i16) * 4u) = m;
  }
  bar_only();
  issue(IC<18>{});
  constexpr float CS = 0.08838834764831845f * 1.4426950408889634f;
  bf16x8 pf[4][3];
#pragma unroll
  for (int j = 0; j < 3; ++j) {
    float m = *(lds_f32*)(L + SCR + ((qg * 4u + 0u) * 48u + 16u * j + i16) * 4u);
#pragma unroll
    for (int u = 1; u < 4; ++u) m = fmaxf(m, *(lds_f32*)(L + SCR + ((qg * 4u + u) * 48u + 16u * j + i16) * 4u));
    mx[j] = m;
    const float nm = -m * CS;
    float sum = 0.f;
#pragma unroll
    for (int t = 0; t < 4; ++t) {
      f32x4 p0, p1;
#pragma unroll
      for (int r = 0; r < 4; ++r) {
        p0[r] = __builtin_amdgcn_exp2f(fmaf(s[t][0][j][r], CS, nm));
        p1[r] = __builtin_amdgcn_exp2f(fmaf(s[t][1][j][r], CS, nm));
        sum += p0[r] + p1[r];
      }
      pf[t][j] = __builtin_shufflevector(cvt4(p0), cvt4(p1), 0, 1, 2, 3, 4, 5, 6, 7);
    }
    sum = xsum16_32(sum);
    if (g == 0) *(lds_f32*)(L + SCR + 2048u + (w * 48u + 16u * j + i16) * 4u) = sum;
  }
  f32x4 o[8][3];
#pragma unroll
  for (int dt = 0; dt < 8; ++dt)
#pragma unroll
    for (int j = 0; j < 3; ++j) o[dt][j] = f32x4{0.f, 0.f, 0.f, 0.f};
  const unsigned fV = 2u * (4u * (g & 1u) + q4);
  const unsigned vrd = (32u * kq + 4u * g + q4) * 256u + (((p4 >> 1) ^ fV) << 4) + 8u * (p4 & 1u);
  static_for<0, 4>([&](auto TI) {
    constexpr int t = decltype(TI)::v;
    constexpr unsigned slot = (unsigned)t * SLOT;
    wait_bar<12 - 4 * t>();
#pragma unroll
    for (int dt = 0; dt < 8; ++dt) {
      const unsigned a = slot + (vrd ^ (dt << 5));
      const bf16x8 vf = ldtr2(L, a, a + 4096u);
#pragma unroll
      for (int j = 0; j < 3; ++j) o[dt][j] = mfma16(vf, pf[t][j], o[dt][j]);
    }
  });
  bar_only();
#pragma unroll
  for (int u = 0; u < 4; ++u) {
    if (u != (int)kq) {
      const unsigned src = (kq - (unsigned)u - 1u) & 3u;
      const unsigned base = ((qg * 4u + u) * 3u + src) * 6144u + l * 16u;
#pragma unroll
      for (int e = 0; e < 2; ++e)
#pragma unroll
        for (int j = 0; j < 3; ++j) *(lds_f32x4*)(L + base + (e * 3 + j) * 1024u) = o[2 * u + e][j];
    }
  }
  f32x4 of[2][3];
  static_for<0, 4>([&](auto UI) {
    constexpr int u = decltype(UI)::v;
    if (u == (int)kq) {
#pragma unroll
      for (int e = 0; e < 2; ++e)
#pragma unroll
        for (int j = 0; j < 3; ++j) of[e][j] = o[2 * u + e][j];
    }
  });
  bar_only();
#pragma unroll
  for (int src = 0; src < 3; ++src) {
    const unsigned base = (w * 3u + src) * 6144u + l * 16u;
#pragma unroll
    for (int e = 0; e < 2; ++e)
#pragma unroll
      for (int j = 0; j < 3; ++j) of[e][j] += *(lds_f32x4*)(L + base + (e * 3 + j) * 1024u);
  }
#pragma unroll
  for (int j = 0; j < 3; ++j) {
    float sum = 0.f;
#pragma unroll
    for (int u = 0; u < 4; ++u) sum += *(lds_f32*)(L + SCR + 2048u + ((qg * 4u + u) * 48u + 16u * j + i16) * 4u);
    const float inv = 1.0f / sum;
    const unsigned q = 48u * qg + 16u * j + i16;
#pragma unroll
    for (int e = 0; e < 2; ++e) {
      const bf16x4 ov = cvt4(of[e][j] * inv);
      *(bf16x4*)(o_ws + (size_t)(b * SQ + q) * HD + h * DH + 32u * kq + 16u * e + 4u * g) = ov;
    }
  }
  (void)mx;
}

__global__ __launch_bounds__(512, 2)
void oproj_kernel(const __bf16* __restrict__ o_ws, const __bf16* __restrict__ wo, float* __restrict__ part) {
  extern __shared__ __attribute__((aligned(16))) char smem[];
  lds_char* L = (lds_char*)smem;
  const unsigned ldsb = (unsigned)(size_t)L;
  const unsigned tid = threadIdx.x, l = tid & 63u;
  unsigned w = __builtin_amdgcn_readfirstlane(tid >> 6);
  asm volatile("s_nop 4" : "+s"(w));
  const unsigned bid = blockIdx.x;
  const unsigned sp = bid & 7u, idx = bid >> 3, mt = idx / 5u, nt = idx % 5u;
  const __amdgpu_buffer_rsrc_t srdO = __builtin_amdgcn_make_buffer_rsrc((void*)(o_ws + (size_t)mt * 128 * HD + sp * 512u), (short)0, (int)((128 * HD - sp * 512u) * 2), 0x00020000);
  const __amdgpu_buffer_rsrc_t srdW = __builtin_amdgcn_make_buffer_rsrc((void*)(wo + (size_t)sp * 512u * DM + nt * 128u), (short)0, (int)((512 * DM - nt * 128u) * 2), 0x00020000);
  const unsigned g = l >> 4, i16 = l & 15u;
  const unsigned voffX = (8u * w + (l >> 3)) * (HD * 2) + (((l & 7u) ^ ((4u * w + g) & 7u)) << 4);
  const unsigned fW = 2u * (g | (((w >> 1) & 1u) << 2));
  const unsigned voffW = (4u * w + g) * (DM * 2) + ((i16 ^ fW) << 4);
  auto issue = [&](auto NI) {
    constexpr int n = decltype(NI)::v;
    constexpr unsigned slot = (unsigned)(n % 5) * SLOT;
    if constexpr (n < 8) {
      dma16(ldsb + slot + w * 1024u, voffW, srdW, (unsigned)n * 64u * (DM * 2));
      dma16(ldsb + slot + w * 1024u + 8192u, voffW, srdW, (unsigned)n * 64u * (DM * 2) + 32u * (DM * 2));
      dma16(ldsb + slot + 16384u + w * 1024u, voffX, srdO, (unsigned)n * 128u);
      dma16(ldsb + slot + 16384u + 8192u + w * 1024u, voffX, srdO, (unsigned)n * 128u + 64u * (HD * 2));
    }
  };
  static_for<0, 5>([&](auto NI) { issue(NI); });
  const unsigned mg = w >> 2, ng = w & 3u;
  const unsigned q4 = i16 >> 2, p4 = l & 3u;
  f32x4 acc[2][4];
#pragma unroll
  for (int e = 0; e < 2; ++e)
#pragma unroll
    for (int j = 0; j < 4; ++j) acc[e][j] = f32x4{0.f, 0.f, 0.f, 0.f};
  const unsigned fWr = 2u * (q4 | ((g & 1u) << 2));
  const unsigned wrd0 = (8u * g + q4) * 256u + (((4u * ng + (p4 >> 1)) ^ fWr) << 4) + 8u * (p4 & 1u);
  const unsigned wrd1 = wrd0 ^ 32u;
  const unsigned xrow = 64u * mg + i16;
  const unsigned xrd0 = 16384u + xrow * 128u + ((g ^ ((xrow >> 1) & 7u)) << 4);
  const unsigned xrd1 = xrd0 ^ 64u;
  static_for<0, 8>([&](auto NI) {
    constexpr int n = decltype(NI)::v;
    constexpr unsigned slot = (unsigned)(n % 5) * SLOT;
    if constexpr (n == 0) wait_bar<16>();
    else if constexpr (n <= 4) wait_bar<12>();
    else wait_bar<4 * (7 - n)>();
    if constexpr (n >= 1) issue(IC<n + 4>{});
#pragma unroll
    for (int ks = 0; ks < 2; ++ks) {
      bf16x8 af[2], bfr[4];
      af[0] = ldtr2(L, slot + wrd0 + ks * 8192u, slot + wrd0 + ks * 8192u + 1024u);
      af[1] = ldtr2(L, slot + wrd1 + ks * 8192u, slot + wrd1 + ks * 8192u + 1024u);
#pragma unroll
      for (int j = 0; j < 4; ++j) bfr[j] = ld128(L, slot + (ks ? xrd1 : xrd0) + j * 2048u);
#pragma unroll
      for (int e = 0; e < 2; ++e)
#pragma unroll
        for (int j = 0; j < 4; ++j) acc[e][j] = mfma16(af[e], bfr[j], acc[e][j]);
    }
  });
#pragma unroll
  for (int e = 0; e < 2; ++e)
#pragma unroll
    for (int j = 0; j < 4; ++j) {
      const unsigned m = mt * 128u + 64u * mg + 16u * j + i16, n = nt * 128u + 32u * ng + 16u * e + 4u * g;
      *(f32x4*)(part + ((size_t)sp * MROWS + m) * DM + n) = acc[e][j];
    }
}

__global__ __launch_bounds__(256)
void reduce_kernel(const float* __restrict__ part, __bf16* __restrict__ out) {
  const unsigned i = (blockIdx.x * 256u + threadIdx.x) * 4u;
  f32x4 a = *(const f32x4*)(part + i);
#pragma unroll
  for (int sp = 1; sp < 8; ++sp) a += *(const f32x4*)(part + (size_t)sp * MROWS * DM + i);
  *(bf16x4*)(out + i) = cvt4(a);
}

extern "C" void kernel_launch(void* const* d_in, const int* in_sizes, int n_in, void* d_out, int out_size, void* d_ws,
                              size_t ws_size, hipStream_t stream) {
  const __bf16* x = (const __bf16*)d_in[0];
  const __bf16* kc = (const __bf16*)d_in[1];
  const __bf16* vc = (const __bf16*)d_in[2];
  const __bf16* wq = (const __bf16*)d_in[3];
  const __bf16* wo = (const __bf16*)d_in[4];
  __bf16* o_ws = (__bf16*)d_ws;
  float* part = (float*)((char*)d_ws + (size_t)MROWS * HD * 2);
  (void)hipFuncSetAttribute((const void*)attn_kernel, hipFuncAttributeMaxDynamicSharedMemorySize, LDS_BYTES);
  (void)hipFuncSetAttribute((const void*)oproj_kernel, hipFuncAttributeMaxDynamicSharedMemorySize, LDS_BYTES);
  hipLaunchKernelGGL(attn_kernel, dim3(256), dim3(512), LDS_BYTES, stream, x, kc, vc, wq, o_ws);
  hipLaunchKernelGGL(oproj_kernel, dim3(240), dim3(512), LDS_BYTES, stream, (const __bf16*)o_ws, wo, part);
  hipLaunchKernelGGL(reduce_kernel, dim3(MROWS * DM / 1024), dim3(256), 0, stream, (const float*)part, (__bf16*)d_out);
}
```

```cpp
#include <hip/hip_runtime.h>
#include <hip/hip_bf16.h>

#define DI __device__ __forceinline__
typedef __attribute__((ext_vector_type(8))) __bf16 bf16x8;
typedef __attribute__((ext_vector_type(4))) __bf16 bf16x4;
typedef __attribute__((ext_vector_type(2))) __bf16 bf16x2;
typedef __attribute__((ext_vector_type(4))) float f32x4;
typedef __attribute__((ext_vector_type(2))) float f32x2;
typedef __attribute__((ext_vector_type(2))) unsigned u32x2;
typedef __attribute__((address_space(3))) char lds_char;
typedef __attribute__((address_space(3))) bf16x8 lds_bf16x8;
typedef __attribute__((address_space(3))) bf16x4 lds_bf16x4;
typedef __attribute__((address_space(3))) f32x4 lds_f32x4;
typedef __attribute__((address_space(3))) float lds_f32;

constexpr int NB = 8, SQ = 96, DM = 640, NH = 32, SK = 512, DH = 128;
constexpr int HD = NH * DH;
constexpr int MROWS = NB * SQ;
constexpr unsigned SLOT = 32768;
constexpr unsigned LDS_BYTES = 5 * SLOT;

DI void dma16(unsigned m0v, unsigned voff, __amdgpu_buffer_rsrc_t srd, unsigned soff) {
  asm volatile("s_mov_b32 m0, %0\n\ts_nop 0\n\tbuffer_load_dwordx4 %1, %2, %3 offen lds"
               :: "s"(m0v), "v"(voff), "s"(srd), "s"(soff) : "memory");
}
template <int N> DI void wait_bar() {
  asm volatile("s_waitcnt vmcnt(%0) lgkmcnt(0)\n\ts_barrier" :: "i"(N) : "memory");
}
template <int N> DI void wait_vm_bar() {
  asm volatile("s_waitcnt vmcnt(%0)\n\ts_barrier" :: "i"(N) : "memory");
}
DI void bar_only() { asm volatile("s_waitcnt lgkmcnt(0)\n\ts_barrier" ::: "memory"); }
DI f32x4 mfma16(bf16x8 a, bf16x8 b, f32x4 c) { return __builtin_amdgcn_mfma_f32_16x16x32_bf16(a, b, c, 0, 0, 0); }
DI bf16x8 ld128(lds_char* L, unsigned off) { return *(lds_bf16x8*)(L + off); }
DI bf16x8 ldtr2(lds_char* L, unsigned off0, unsigned off1) {
  bf16x4 a = __builtin_amdgcn_ds_read_tr16_b64_v4bf16((lds_bf16x4*)(L + off0));
  bf16x4 b = __builtin_amdgcn_ds_read_tr16_b64_v4bf16((lds_bf16x4*)(L + off1));
  return __builtin_shufflevector(a, b, 0, 1, 2, 3, 4, 5, 6, 7);
}
DI bf16x4 cvt4(f32x4 v) { return __builtin_convertvector(v, bf16x4); }
DI float xmax16_32(float m) {
  auto r = __builtin_amdgcn_permlane16_swap(__float_as_uint(m), __float_as_uint(m), false, false);
  m = fmaxf(__uint_as_float(r[0]), __uint_as_float(r[1]));
  auto r2 = __builtin_amdgcn_permlane32_swap(__float_as_uint(m), __float_as_uint(m), false, false);
  return fmaxf(__uint_as_float(r2[0]), __uint_as_float(r2[1]));
}
DI float xsum16_32(float m) {
  auto r = __builtin_amdgcn_permlane16_swap(__float_as_uint(m), __float_as_uint(m), false, false);
  m = __uint_as_float(r[0]) + __uint_as_float(r[1]);
  auto r2 = __builtin_amdgcn_permlane32_swap(__float_as_uint(m), __float_as_uint(m), false, false);
  return __uint_as_float(r2[0]) + __uint_as_float(r2[1]);
}

#define STAMP(i)
#define STAMPC(i)
#define STAMP_DECL
#define STAMP_OUT(base)
template <int I> struct IC { static constexpr int v = I; };
template <int I0, int I1, class F> DI void static_for(F&& f) {
  if constexpr (I0 < I1) { f(IC<I0>{}); static_for<I0 + 1, I1>(f); }
}

DI void dma16nt(unsigned m0v, unsigned voff, __amdgpu_buffer_rsrc_t srd, unsigned soff) {
  asm volatile("s_mov_b32 m0, %0\n\ts_nop 0\n\tbuffer_load_dwordx4 %1, %2, %3 offen nt lds"
               :: "s"(m0v), "v"(voff), "s"(srd), "s"(soff) : "memory");
}
DI void dma16lo(unsigned m0v, unsigned voff, __amdgpu_buffer_rsrc_t srd, unsigned soff) {
  asm volatile("s_mov_b32 m0, %0\n\ts_mov_b32 exec_hi, 0\n\tbuffer_load_dwordx4 %1, %2, %3 offen lds\n\ts_mov_b32 exec_hi, -1"
               :: "s"(m0v), "v"(voff), "s"(srd), "s"(soff) : "memory");
}
#define SB() __builtin_amdgcn_sched_barrier(0)
#define EARLYK 2

__global__ __launch_bounds__(512, 2)
void attn_kernel(const __bf16* __restrict__ x, const __bf16* __restrict__ kc, const __bf16* __restrict__ vc,
                 const __bf16* __restrict__ wq, __bf16* __restrict__ o_ws, unsigned* __restrict__ cnt) {
  extern __shared__ __attribute__((aligned(16))) char smem[];
  lds_char* L = (lds_char*)smem;
  const unsigned ldsb = (unsigned)(size_t)L;
  const unsigned tid = threadIdx.x, l = tid & 63u;
  unsigned w = __builtin_amdgcn_readfirstlane(tid >> 6);
  asm volatile("s_nop 4" : "+s"(w));
  const unsigned bid = blockIdx.x;
  const unsigned hh = (bid >> 3) & 3u, h = (bid & 7u) * 4u + hh, b = bid >> 5;
  const __amdgpu_buffer_rsrc_t srdX = __builtin_amdgcn_make_buffer_rsrc((void*)(x + (size_t)b * SQ * DM), (short)0, SQ * DM * 2, 0x00020000);
  const __amdgpu_buffer_rsrc_t srdW = __builtin_amdgcn_make_buffer_rsrc((void*)(wq + h * DH), (short)0, (int)((DM * HD - h * DH) * 2), 0x00020000);
  const __amdgpu_buffer_rsrc_t srdK = __builtin_amdgcn_make_buffer_rsrc((void*)(kc + (size_t)(b * NH + h) * SK * DH), (short)0, SK * DH * 2, 0x00020000);
  const __amdgpu_buffer_rsrc_t srdV = __builtin_amdgcn_make_buffer_rsrc((void*)(vc + (size_t)(b * NH + h) * SK * DH), (short)0, SK * DH * 2, 0x00020000);
  const unsigned g = l >> 4, i16 = l & 15u;
  const unsigned xr0 = 12u * w + (l >> 3), xr1 = xr0 + 8u;
  const unsigned voffX0 = xr0 * (DM * 2) + (((l & 7u) ^ ((xr0 >> 1) & 7u)) << 4);
  const unsigned voffX1 = xr1 * (DM * 2) + (((l & 7u) ^ ((xr1 >> 1) & 7u)) << 4);
  const unsigned fW = 2u * (g | (((w >> 1) & 1u) << 2));
  const unsigned voffW = (4u * w + g) * (HD * 2) + ((i16 ^ fW) << 4);
  const unsigned voffK = (4u * w + g) * 256u + ((i16 ^ ((4u * w + g) & 15u)) << 4);
  const unsigned voffV = (4u * w + g) * 256u + ((i16 ^ (2u * ((4u * w + g) & 7u))) << 4);
  constexpr unsigned QIMG = 2 * SLOT, SCR = QIMG + 24576u;
  const unsigned rot = b + ((hh * 5u + 1u) >> 1);
  auto ktile = [&](int n) { unsigned k = rot + (unsigned)n; k = k >= 20u ? k - 20u : k; return k >= 10u ? k - 10u : k; };
  auto xw_piece = [&](auto NI, auto PI) {
    constexpr int n = decltype(NI)::v, pc = decltype(PI)::v;
    constexpr unsigned slot = (unsigned)(1 + n % 4) * SLOT;
    const unsigned kt = ktile(n);
    if constexpr (pc == 0) dma16(ldsb + slot + w * 1024u, voffW, srdW, kt * 64u * (HD * 2));
    if constexpr (pc == 1) dma16(ldsb + slot + w * 1024u + 8192u, voffW, srdW, kt * 64u * (HD * 2) + 32u * (HD * 2));
    if constexpr (pc == 2) dma16(ldsb + slot + 16384u + w * 1536u, voffX0, srdX, kt * 128u);
    if constexpr (pc == 3) dma16lo(ldsb + slot + 16384u + w * 1536u + 1024u, voffX1, srdX, kt * 128u);
  };
  auto kv_piece = [&](auto PI) {
    constexpr int p = decltype(PI)::v, t = (p >> 2) & 3, j = p & 3;
    constexpr unsigned slot = (unsigned)(t == 0 ? 0 : t == 1 ? 3 : t == 2 ? 4 : 1) * SLOT;
    if constexpr (p < 16) dma16nt(ldsb + slot + j * 8192u + w * 1024u, voffK, srdK, (unsigned)t * SLOT + j * 8192u);
    else if constexpr (p < 32) dma16nt(ldsb + slot + j * 8192u + w * 1024u, voffV, srdV, (unsigned)t * SLOT + j * 8192u);
  };
  STAMP_DECL
  STAMP(0); STAMPC(28);
  static_for<0, 4>([&](auto PI) { xw_piece(IC<0>{}, PI); });
  static_for<0, 4>([&](auto PI) { xw_piece(IC<1>{}, PI); });
  static_for<0, 2 * EARLYK>([&](auto PI) { kv_piece(PI); });
  static_for<0, 4>([&](auto PI) { xw_piece(IC<2>{}, PI); });

  const unsigned qg = w >> 2, kq = w & 3u;
  const unsigned q4 = i16 >> 2, p4 = l & 3u;
  f32x4 qacc[2][3];
#pragma unroll
  for (int e = 0; e < 2; ++e)
#pragma unroll
    for (int j = 0; j < 3; ++j) qacc[e][j] = f32x4{0.f, 0.f, 0.f, 0.f};
  constexpr int KVPRE = 8;
  {
    const unsigned fWr = 2u * (q4 | ((g & 1u) << 2));
    const unsigned wrd0 = (8u * g + q4) * 256u + (((4u * kq + (p4 >> 1)) ^ fWr) << 4) + 8u * (p4 & 1u);
    const unsigned wrd1 = wrd0 ^ 32u;
    const unsigned xrow = 48u * qg + i16;
    const unsigned xrd0 = 16384u + xrow * 128u + ((g ^ ((xrow >> 1) & 7u)) << 4);
    const unsigned xrd1 = xrd0 ^ 64u;
    bf16x8 a0[2], b0[3], a1[2], b1[3];
    auto stream = [&](auto CI) {
      constexpr int c = decltype(CI)::v;
      if constexpr ((c >> 2) <= 9) xw_piece(IC<(c >> 2)>{}, IC<(c & 3)>{});
      else if constexpr (c - 40 + 2 * EARLYK < KVPRE) kv_piece(IC<c - 40 + 2 * EARLYK>{});
    };
    auto step = [&](bf16x8 (&ca)[2], bf16x8 (&cb)[3], bf16x8 (&na)[2], bf16x8 (&nb)[3], auto SLOTI, auto KSI, auto RDI, auto C0I) {
      constexpr unsigned slot = (unsigned)decltype(SLOTI)::v * SLOT;
      constexpr int ks = decltype(KSI)::v, c0 = decltype(C0I)::v;
      constexpr bool RD = decltype(RDI)::v;
      const unsigned xr = slot + (ks ? xrd1 : xrd0);
      qacc[0][0] = mfma16(ca[0], cb[0], qacc[0][0]); SB();
      if constexpr (RD) na[0] = ldtr2(L, slot + wrd0 + ks * 8192u, slot + wrd0 + ks * 8192u + 1024u);
      SB();
      qacc[0][1] = mfma16(ca[0], cb[1], qacc[0][1]); SB();
      if constexpr (RD) nb[0] = ld128(L, xr);
      SB();
      qacc[0][2] = mfma16(ca[0], cb[2], qacc[0][2]); SB();
      if constexpr (RD) nb[1] = ld128(L, xr + 2048u);
      if constexpr (c0 >= 0) stream(IC<(c0 >= 0 ? c0 : 0)>{});
      SB();
      qacc[1][0] = mfma16(ca[1], cb[0], qacc[1][0]); SB();
      if constexpr (RD) nb[2] = ld128(L, xr + 4096u);
      SB();
      qacc[1][1] = mfma16(ca[1], cb[1], qacc[1][1]); SB();
      if constexpr (RD) na[1] = ldtr2(L, slot + wrd1 + ks * 8192u, slot + wrd1 + ks * 8192u + 1024u);
      SB();
      qacc[1][2] = mfma16(ca[1], cb[2], qacc[1][2]); SB();
      if constexpr (c0 >= 0) stream(IC<(c0 >= 0 ? c0 + 1 : 0)>{});
      SB();
    };
    wait_vm_bar<8 + 2 * EARLYK>(); STAMP(1);
    a0[0] = ldtr2(L, SLOT + wrd0, SLOT + wrd0 + 1024u);
#pragma unroll
    for (int j = 0; j < 3; ++j) b0[j] = ld128(L, SLOT + xrd0 + j * 2048u);
    a0[1] = ldtr2(L, SLOT + wrd1, SLOT + wrd1 + 1024u);
    SB();
    static_for<0, 10>([&](auto NI) {
      constexpr int n = decltype(NI)::v;
      step(a0, b0, a1, b1, IC<1 + n % 4>{}, IC<1>{}, IC<1>{}, IC<(n >= 1 ? 4 * (n + 2) + 2 : -1)>{});
      if constexpr (n == 0) wait_vm_bar<4 + 2 * EARLYK>();
      else if constexpr (n < 8) wait_vm_bar<4>();
      else if constexpr (n == 8) wait_vm_bar<4>();
      else bar_only();
      if constexpr (n == 2 || n == 7) { STAMP(2 + n); }
      SB();
      step(a1, b1, a0, b0, IC<1 + (n + 1) % 4>{}, IC<0>{}, IC<(n < 9)>{}, IC<4 * (n + 3)>{});
    });
  }
#pragma unroll
  for (int e = 0; e < 2; ++e)
#pragma unroll
    for (int j = 0; j < 3; ++j) {
      const unsigned q = 48u * qg + 16u * j + i16, c = 2u * (2u * kq + e) + (g >> 1);
      *(lds_bf16x4*)(L + QIMG + q * 256u + ((c ^ i16) << 4) + 8u * (g & 1u)) = cvt4(qacc[e][j]);
    }
  STAMP(12);
  f32x4 s[4][2][3];
  const unsigned krd = (32u * kq + i16) * 256u + ((g ^ i16) << 4);
  const unsigned qrd = QIMG + (48u * qg + i16) * 256u + ((g ^ i16) << 4);
  float mx[3];
  {
    bf16x8 qf[3][4];
    static_for<0, 4>([&](auto TI) {
      constexpr int t = decltype(TI)::v;
      constexpr unsigned slot = (unsigned)(t == 0 ? 0 : t == 1 ? 3 : t == 2 ? 4 : 1) * SLOT;
      if constexpr (t == 0) wait_bar<4>(); else wait_vm_bar<4>();
      STAMP(13 + t);
      bf16x8 kfr[8];
      kfr[0] = ld128(L, slot + krd);
      if constexpr (t == 0) {
#pragma unroll
        for (int j = 0; j < 3; ++j) qf[j][0] = ld128(L, qrd + j * 4096u);
      }
      kfr[1] = ld128(L, slot + (krd ^ 64u));
      if constexpr (t == 0) {
#pragma unroll
        for (int j = 0; j < 3; ++j) qf[j][1] = ld128(L, (qrd ^ 64u) + j * 4096u);
      }
      kv_piece(IC<4 * t + 8>{});
      SB();
      static_for<0, 8>([&](auto FI) {
        constexpr int f = decltype(FI)::v, kt = f >> 2, ks = f & 3;
        if constexpr (f + 2 < 8) kfr[f + 2] = ld128(L, slot + (krd ^ (((f + 2) & 3) << 6)) + ((f + 2) >> 2) * 4096u);
        if constexpr (t == 0 && f < 2) {
#pragma unroll
          for (int j = 0; j < 3; ++j) qf[j][f + 2] = ld128(L, (qrd ^ ((f + 2) << 6)) + j * 4096u);
        }
        SB();
#pragma unroll
        for (int j = 0; j < 3; ++j) s[t][kt][j] = mfma16(kfr[f], qf[j][ks], ks == 0 ? f32x4{0.f, 0.f, 0.f, 0.f} : s[t][kt][j]);
        if constexpr (f == 3) kv_piece(IC<4 * t + 9>{});
        SB();
      });
#pragma unroll
      for (int j = 0; j < 3; ++j) {
        float m = fmaxf(fmaxf(s[t][0][j][0], s[t][0][j][1]), fmaxf(s[t][0][j][2], s[t][0][j][3]));
        m = fmaxf(m, fmaxf(fmaxf(s[t][1][j][0], s[t][1][j][1]), fmaxf(s[t][1][j][2], s[t][1][j][3])));
        mx[j] = t == 0 ? m : fmaxf(mx[j], m);
      }
      SB();
      if constexpr (t < 3) {
        asm volatile("s_waitcnt vmcnt(4)" ::: "memory");
        kv_piece(IC<4 * t + 10>{}); kv_piece(IC<4 * t + 11>{});
      }
    });
  }
#pragma unroll
  for (int j = 0; j < 3; ++j) {
    const float m = xmax16_32(mx[j]);
    if (g == 0) *(lds_f32*)(L + SCR + (w * 48u + 16u * j + i16) * 4u) = m;
  }
  bar_only(); STAMP(17); STAMPC(29);
  constexpr float CS = 0.08838834764831845f * 1.4426950408889634f;
  bf16x8 pf[4][3];
  const __bf16 one = (__bf16)1.0f;
  const bf16x8 ones = {one, one, one, one, one, one, one, one};
  float mrow[3];
#pragma unroll
  for (int j = 0; j < 3; ++j) {
    float m = *(lds_f32*)(L + SCR + ((qg * 4u + 0u) * 48u + 16u * j + i16) * 4u);
#pragma unroll
    for (int u = 1; u < 4; ++u) m = fmaxf(m, *(lds_f32*)(L + SCR + ((qg * 4u + u) * 48u + 16u * j + i16) * 4u));
    mrow[j] = -m * CS;
  }
  static_for<0, 3>([&](auto JI) {
    constexpr int j = decltype(JI)::v;
    const f32x2 nm = {mrow[j], mrow[j]}, cs2 = {CS, CS};
    f32x4 ls = {0.f, 0.f, 0.f, 0.f};
    static_for<0, 4>([&](auto TI) {
      constexpr int t = decltype(TI)::v;
      if constexpr ((t & 1) == 0 && 2 * j + (t >> 1) < 4) { SB(); kv_piece(IC<22 + 2 * j + (t >> 1)>{}); SB(); }
      f32x4 p0, p1;
#pragma unroll
      for (int r = 0; r < 4; r += 2) {
        const f32x2 u0 = __builtin_elementwise_fma(f32x2{s[t][0][j][r], s[t][0][j][r + 1]}, cs2, nm);
        const f32x2 u1 = __builtin_elementwise_fma(f32x2{s[t][1][j][r], s[t][1][j][r + 1]}, cs2, nm);
        p0[r] = __builtin_amdgcn_exp2f(u0[0]); p0[r + 1] = __builtin_amdgcn_exp2f(u0[1]);
        p1[r] = __builtin_amdgcn_exp2f(u1[0]); p1[r + 1] = __builtin_amdgcn_exp2f(u1[1]);
      }
      pf[t][j] = __builtin_shufflevector(cvt4(p0), cvt4(p1), 0, 1, 2, 3, 4, 5, 6, 7);
      asm volatile("" : "+v"(pf[t][j]));
      ls = mfma16(ones, pf[t][j], ls);
    });
    if (g == 0) *(lds_f32*)(L + SCR + 2048u + (w * 48u + 16u * j + i16) * 4u) = ls[0];
    SB();
  });
  f32x4 o[8][3];
  const unsigned fV = 2u * (4u * (g & 1u) + q4);
  const unsigned vrd = (32u * kq + 4u * g + q4) * 256u + (((p4 >> 1) ^ fV) << 4) + 8u * (p4 & 1u);
  const unsigned vrk = vrd ^ (kq << 6);
  float inv[3];
  STAMP(18);
  static_for<0, 4>([&](auto TI) {
    constexpr int t = decltype(TI)::v;
    constexpr unsigned slot = (unsigned)(t == 0 ? 0 : t == 1 ? 3 : t == 2 ? 4 : 1) * SLOT;
    if constexpr (t == 0) wait_bar<6>(); else wait_vm_bar<(t == 1 ? 4 : t == 2 ? 4 : 0)>();
    STAMP(19 + t);
    bf16x8 vf[8];
    auto rdv = [&](int v) { const int i = (v + 2) & 7; const unsigned a = slot + (vrk ^ (i << 5)); vf[v] = ldtr2(L, a, a + 4096u); };
    rdv(0); rdv(1);
    if constexpr (t == 0) {
#pragma unroll
      for (int j = 0; j < 3; ++j) {
        float sum = 0.f;
#pragma unroll
        for (int u = 0; u < 4; ++u) sum += *(lds_f32*)(L + SCR + 2048u + ((qg * 4u + u) * 48u + 16u * j + i16) * 4u);
        inv[j] = 1.0f / sum;
      }
    }
    SB();
    static_for<0, 8>([&](auto VI) {
      constexpr int v = decltype(VI)::v, i = (v + 2) & 7;
      if constexpr (v + 2 < 8) rdv(v + 2);
      if constexpr (t == 0 && (v == 2 || v == 6)) kv_piece(IC<26 + (v >> 2)>{});
      if constexpr (t == 1 && (v == 0 || v == 4)) kv_piece(IC<28 + (v >> 2)>{});
      SB();
#pragma unroll
      for (int j = 0; j < 3; ++j) o[i][j] = mfma16(vf[v], pf[t][j], t == 0 ? f32x4{0.f, 0.f, 0.f, 0.f} : o[i][j]);
      SB();
      if constexpr (t == 3 && v >= 3 && (v & 1)) {
        constexpr int pr = (v - 3) / 2 + 1;
        const unsigned rbase = (pr == 1 ? 0u : pr == 2 ? 3u * SLOT : 4u * SLOT) + (qg * 4u + (kq ^ (unsigned)pr)) * 3072u + l * 16u;
#pragma unroll
        for (int j = 0; j < 3; ++j) {
          const bf16x8 pk = __builtin_shufflevector(cvt4(o[2 * pr][j] * inv[j]), cvt4(o[2 * pr + 1][j] * inv[j]), 0, 1, 2, 3, 4, 5, 6, 7);
          *(lds_bf16x8*)(L + rbase + j * 1024u) = pk;
        }
        SB();
      }
    });
    if constexpr (t == 1) {
      asm volatile("s_waitcnt vmcnt(4)" ::: "memory");
      kv_piece(IC<30>{}); kv_piece(IC<31>{});
    }
  });
  bar_only(); STAMP(23); STAMPC(30);
  if (tid == 0 && bid < 30u) cnt[bid * 32u] = 0u;
  {
    bf16x8 rr[3][3];
#pragma unroll
    for (int pr = 1; pr < 4; ++pr) {
      const unsigned rbase = (pr == 1 ? 0u : pr == 2 ? 3u * SLOT : 4u * SLOT) + w * 3072u + l * 16u;
#pragma unroll
      for (int j = 0; j < 3; ++j) rr[pr - 1][j] = ld128(L, rbase + j * 1024u);
    }
#pragma unroll
    for (int j = 0; j < 3; ++j) {
      const unsigned q = 48u * qg + 16u * j + i16;
#pragma unroll
      for (int e = 0; e < 2; ++e) {
        f32x4 acc = o[e][j] * inv[j];
#pragma unroll
        for (int pr = 0; pr < 3; ++pr)
#pragma unroll
          for (int r = 0; r < 4; ++r) acc[r] += (float)rr[pr][j][4 * e + r];
        *(bf16x4*)(o_ws + (size_t)(b * SQ + q) * HD + h * DH + 32u * kq + 16u * e + 4u * g) = cvt4(acc);
      }
    }
  }
  STAMP(25); STAMPC(31);
  STAMP_OUT((char*)o_ws + (64u << 20));
}

typedef __attribute__((ext_vector_type(4))) unsigned u32x4;
constexpr size_t WS_O = 0, WS_PART = (size_t)MROWS * HD * 2, WS_CNT = WS_PART + (size_t)30 * 64 * 4096;

__global__ __launch_bounds__(512, 2)
void oproj_kernel(const __bf16* __restrict__ o_ws, const __bf16* __restrict__ wo, char* __restrict__ part,
                  unsigned* __restrict__ cnt, __bf16* __restrict__ out) {
  extern __shared__ __attribute__((aligned(16))) char smem[];
  lds_char* L = (lds_char*)smem;
  const unsigned ldsb = (unsigned)(size_t)L;
  const unsigned tid = threadIdx.x, l = tid & 63u;
  unsigned w = __builtin_amdgcn_readfirstlane(tid >> 6);
  asm volatile("s_nop 4" : "+s"(w));
  const unsigned bid = blockIdx.x;
  const unsigned sp = bid & 7u, idx = bid >> 3, mt = idx / 5u, nt = idx % 5u;
  const __amdgpu_buffer_rsrc_t srdO = __builtin_amdgcn_make_buffer_rsrc((void*)(o_ws + (size_t)mt * 128 * HD + sp * 512u), (short)0, (int)((128 * HD - sp * 512u) * 2), 0x00020000);
  const __amdgpu_buffer_rsrc_t srdW = __builtin_amdgcn_make_buffer_rsrc((void*)(wo + (size_t)sp * 512u * DM + nt * 128u), (short)0, (int)((512 * DM - nt * 128u) * 2), 0x00020000);
  const unsigned g = l >> 4, i16 = l & 15u;
  const unsigned voffX = (8u * w + (l >> 3)) * (HD * 2) + (((l & 7u) ^ ((4u * w + g) & 7u)) << 4);
  const unsigned fW = 2u * (g | (((w >> 1) & 1u) << 2));
  const unsigned voffW = (4u * w + g) * (DM * 2) + ((i16 ^ fW) << 4);
  const unsigned rot = idx & 7u;
  auto stream = [&](auto CI) {
    constexpr int c = decltype(CI)::v, n = c >> 2, pc = c & 3;
    constexpr unsigned slot = (unsigned)(n % 4) * SLOT;
    if constexpr (n < 8) {
      const unsigned kt = (rot + (unsigned)n) & 7u;
      if constexpr (pc == 0) dma16(ldsb + slot + w * 1024u, voffW, srdW, kt * 64u * (DM * 2));
      if constexpr (pc == 1) dma16(ldsb + slot + w * 1024u + 8192u, voffW, srdW, kt * 64u * (DM * 2) + 32u * (DM * 2));
      if constexpr (pc == 2) dma16(ldsb + slot + 16384u + w * 1024u, voffX, srdO, kt * 128u);
      if constexpr (pc == 3) dma16(ldsb + slot + 16384u + 8192u + w * 1024u, voffX, srdO, kt * 128u + 64u * (HD * 2));
    }
  };
  STAMP_DECL STAMP(0);
  static_for<0, 12>([&](auto CI) { stream(CI); });
  const unsigned mg = w >> 2, ng = w & 3u;
  const unsigned q4 = i16 >> 2, p4 = l & 3u;
  f32x4 acc[2][4];
#pragma unroll
  for (int e = 0; e < 2; ++e)
#pragma unroll
    for (int j = 0; j < 4; ++j) acc[e][j] = f32x4{0.f, 0.f, 0.f, 0.f};
  const unsigned fWr = 2u * (q4 | ((g & 1u) << 2));
  const unsigned wrd0 = (8u * g + q4) * 256u + (((4u * ng + (p4 >> 1)) ^ fWr) << 4) + 8u * (p4 & 1u);
  const unsigned wrd1 = wrd0 ^ 32u;
  const unsigned xrow = 64u * mg + i16;
  const unsigned xrd0 = 16384u + xrow * 128u + ((g ^ ((xrow >> 1) & 7u)) << 4);
  const unsigned xrd1 = xrd0 ^ 64u;
  bf16x8 a0[2], b0[4], a1[2], b1[4];
  auto step = [&](bf16x8 (&ca)[2], bf16x8 (&cb)[4], bf16x8 (&na)[2], bf16x8 (&nb)[4], auto SLOTI, auto KSI, auto RDI, auto C0I) {
    constexpr unsigned slot = (unsigned)decltype(SLOTI)::v * SLOT;
    constexpr int ks = decltype(KSI)::v, c0 = decltype(C0I)::v;
    constexpr bool RD = decltype(RDI)::v;
    const unsigned xr = slot + (ks ? xrd1 : xrd0);
    acc[0][0] = mfma16(ca[0], cb[0], acc[0][0]); SB();
    if constexpr (RD) na[0] = ldtr2(L, slot + wrd0 + ks * 8192u, slot + wrd0 + ks * 8192u + 1024u);
    SB();
    acc[0][1] = mfma16(ca[0], cb[1], acc[0][1]); SB();
    if constexpr (RD) nb[0] = ld128(L, xr);
    SB();
    acc[0][2] = mfma16(ca[0], cb[2], acc[0][2]); SB();
    if constexpr (RD) nb[1] = ld128(L, xr + 2048u);
    SB();
    acc[0][3] = mfma16(ca[0], cb[3], acc[0][3]); SB();
    if constexpr (RD) nb[2] = ld128(L, xr + 4096u);
    if constexpr (c0 >= 0) stream(IC<(c0 >= 0 ? c0 : 0)>{});
    SB();
    acc[1][0] = mfma16(ca[1], cb[0], acc[1][0]); SB();
    if constexpr (RD) nb[3] = ld128(L, xr + 6144u);
    SB();
    acc[1][1] = mfma16(ca[1], cb[1], acc[1][1]); SB();
    if constexpr (RD) na[1] = ldtr2(L, slot + wrd1 + ks * 8192u, slot + wrd1 + ks * 8192u + 1024u);
    SB();
    acc[1][2] = mfma16(ca[1], cb[2], acc[1][2]);
    acc[1][3] = mfma16(ca[1], cb[3], acc[1][3]); SB();
    if constexpr (c0 >= 0) stream(IC<(c0 >= 0 ? c0 + 1 : 0)>{});
    SB();
  };
  wait_vm_bar<8>(); STAMP(1);
  a0[0] = ldtr2(L, wrd0, wrd0 + 1024u);
#pragma unroll
  for (int j = 0; j < 4; ++j) b0[j] = ld128(L, xrd0 + j * 2048u);
  a0[1] = ldtr2(L, wrd1, wrd1 + 1024u);
  SB();
  static_for<0, 8>([&](auto NI) {
    constexpr int n = decltype(NI)::v;
    step(a0, b0, a1, b1, IC<n % 4>{}, IC<1>{}, IC<1>{}, IC<(n >= 1 ? 4 * (n + 2) + 2 : -1)>{});
    if constexpr (n < 6) wait_vm_bar<4>();
    else if constexpr (n == 6) wait_vm_bar<0>();
    if constexpr (n == 2 || n == 6) { STAMP(2 + n); }
    SB();
    step(a1, b1, a0, b0, IC<(n + 1) % 4>{}, IC<0>{}, IC<(n < 7)>{}, IC<4 * (n + 3)>{});
  });
  STAMP(10);
  const __amdgpu_buffer_rsrc_t srdP = __builtin_amdgcn_make_buffer_rsrc((void*)(part + (size_t)idx * 64 * 4096), (short)0, 64 * 4096, 0x00020000);
#pragma unroll
  for (int j = 0; j < 4; ++j) {
    const unsigned dst = 4u * mg + j;
    if (dst != sp) {
      const bf16x8 pk = __builtin_shufflevector(cvt4(acc[0][j]), cvt4(acc[1][j]), 0, 1, 2, 3, 4, 5, 6, 7);
      __builtin_amdgcn_raw_buffer_store_b128(__builtin_bit_cast(u32x4, pk), srdP, l * 16u, (dst * 8u + sp) * 4096u + ng * 1024u, 16);
    }
  }
  asm volatile("s_waitcnt vmcnt(0)\n\ts_barrier" ::: "memory");
  STAMP(11);
  if (tid == 0) __hip_atomic_fetch_add(cnt + idx * 32u, 1u, __ATOMIC_RELAXED, __HIP_MEMORY_SCOPE_AGENT);
  if (mg == (sp >> 2)) {
    for (unsigned spin = 0; spin < (1u << 22); ++spin) {
      const unsigned c = __hip_atomic_load(cnt + idx * 32u, __ATOMIC_RELAXED, __HIP_MEMORY_SCOPE_AGENT);
      if (__builtin_amdgcn_readfirstlane(c) >= 8u) break;
      __builtin_amdgcn_s_sleep(1);
    }
    asm volatile("" ::: "memory");
    STAMP(12);
    u32x4 r[7];
#pragma unroll
    for (int i = 0; i < 7; ++i) {
      const unsigned src = (sp + 1u + i) & 7u;
      r[i] = __builtin_amdgcn_raw_buffer_load_b128(srdP, l * 16u, (sp * 8u + src) * 4096u + ng * 1024u, 16);
    }
    f32x4 f0, f1;
    static_for<0, 4>([&](auto JI) {
      constexpr int j = decltype(JI)::v;
      if ((sp & 3u) == (unsigned)j) { f0 = acc[0][j]; f1 = acc[1][j]; }
    });
#pragma unroll
    for (int i = 0; i < 7; ++i) {
      const bf16x8 v = __builtin_bit_cast(bf16x8, r[i]);
#pragma unroll
      for (int q = 0; q < 4; ++q) { f0[q] += (float)v[q]; f1[q] += (float)v[4 + q]; }
    }
    const unsigned m = mt * 128u + 16u * sp + i16, n = nt * 128u + 32u * ng + 4u * g;
    *(bf16x4*)(out + (size_t)m * DM + n) = cvt4(f0);
    *(bf16x4*)(out + (size_t)m * DM + n + 16u) = cvt4(f1);
  }
}

extern "C" void kernel_launch(void* const* d_in, const int* in_sizes, int n_in, void* d_out, int out_size, void* d_ws,
                              size_t ws_size, hipStream_t stream) {
  const __bf16* x = (const __bf16*)d_in[0];
  const __bf16* kc = (const __bf16*)d_in[1];
  const __bf16* vc = (const __bf16*)d_in[2];
  const __bf16* wq = (const __bf16*)d_in[3];
  const __bf16* wo = (const __bf16*)d_in[4];
  __bf16* o_ws = (__bf16*)((char*)d_ws + WS_O);
  char* part = (char*)d_ws + WS_PART;
  unsigned* cnt = (unsigned*)((char*)d_ws + WS_CNT);
  (void)hipFuncSetAttribute((const void*)attn_kernel, hipFuncAttributeMaxDynamicSharedMemorySize, LDS_BYTES);
  (void)hipFuncSetAttribute((const void*)oproj_kernel, hipFuncAttributeMaxDynamicSharedMemorySize, 4 * SLOT);
  hipLaunchKernelGGL(attn_kernel, dim3(256), dim3(512), LDS_BYTES, stream, x, kc, vc, wq, o_ws, cnt);
  hipLaunchKernelGGL(oproj_kernel, dim3(240), dim3(512), 4 * SLOT, stream, (const __bf16*)o_ws, wo, part, cnt, (__bf16*)d_out);
}
```

```cpp
#include <hip/hip_runtime.h>
#include <hip/hip_bf16.h>

#define DI __device__ __forceinline__
typedef __attribute__((ext_vector_type(8))) __bf16 bf16x8;
typedef __attribute__((ext_vector_type(4))) __bf16 bf16x4;
typedef __attribute__((ext_vector_type(4))) float f32x4;
typedef __attribute__((ext_vector_type(2))) float f32x2;
typedef __attribute__((ext_vector_type(4))) unsigned u32x4;
typedef __attribute__((address_space(3))) char lds_char;
typedef __attribute__((address_space(3))) bf16x8 lds_bf16x8;
typedef __attribute__((address_space(3))) bf16x4 lds_bf16x4;
typedef __attribute__((address_space(3))) f32x4 lds_f32x4;
typedef __attribute__((address_space(3))) float lds_f32;
typedef __amdgpu_buffer_rsrc_t srd_t;

constexpr int NB = 8, SQ = 96, DM = 640, NH = 32, SK = 512, DH = 128;
constexpr int HD = NH * DH;
constexpr unsigned SLOT = 32768;
constexpr unsigned LDS_BYTES = 5 * SLOT;
constexpr unsigned WS_SYNC = 0;
constexpr unsigned WS_O = 65536;
constexpr unsigned WS_P = WS_O + 256u * 24576u;
constexpr unsigned WS_END = WS_P + 256u * 32768u;

#define SB() __builtin_amdgcn_sched_barrier(0)
DI void dma16(unsigned m0v, unsigned voff, srd_t srd, unsigned soff) {
  asm volatile("s_mov_b32 m0, %0\n\ts_nop 0\n\tbuffer_load_dwordx4 %1, %2, %3 offen lds"
               :: "s"(m0v), "v"(voff), "s"(srd), "s"(soff) : "memory");
}
DI void dma16nt(unsigned m0v, unsigned voff, srd_t srd, unsigned soff) {
  asm volatile("s_mov_b32 m0, %0\n\ts_nop 0\n\tbuffer_load_dwordx4 %1, %2, %3 offen nt lds"
               :: "s"(m0v), "v"(voff), "s"(srd), "s"(soff) : "memory");
}
DI void dma16lo(unsigned m0v, unsigned voff, srd_t srd, unsigned soff) {
  asm volatile("s_mov_b32 m0, %0\n\ts_mov_b32 exec_hi, 0\n\tbuffer_load_dwordx4 %1, %2, %3 offen lds\n\ts_mov_b32 exec_hi, -1"
               :: "s"(m0v), "v"(voff), "s"(srd), "s"(soff) : "memory");
}
template <int N> DI void wait_bar() { asm volatile("s_waitcnt vmcnt(%0) lgkmcnt(0)\n\ts_barrier" :: "i"(N) : "memory"); }
template <int N> DI void wait_vm_bar() { asm volatile("s_waitcnt vmcnt(%0)\n\ts_barrier" :: "i"(N) : "memory"); }
template <int N> DI void wait_vm() { asm volatile("s_waitcnt vmcnt(%0)" :: "i"(N) : "memory"); }
DI void bar_only() { asm volatile("s_waitcnt lgkmcnt(0)\n\ts_barrier" ::: "memory"); }
DI f32x4 mfma16(bf16x8 a, bf16x8 b, f32x4 c) { return __builtin_amdgcn_mfma_f32_16x16x32_bf16(a, b, c, 0, 0, 0); }
DI bf16x8 ld128(lds_char* L, unsigned off) { return *(lds_bf16x8*)(L + off); }
DI bf16x8 ldtr2(lds_char* L, unsigned off0, unsigned off1) {
  bf16x4 a = __builtin_amdgcn_ds_read_tr16_b64_v4bf16((lds_bf16x4*)(L + off0));
  bf16x4 b = __builtin_amdgcn_ds_read_tr16_b64_v4bf16((lds_bf16x4*)(L + off1));
  return __builtin_shufflevector(a, b, 0, 1, 2, 3, 4, 5, 6, 7);
}
DI bf16x4 cvt4(f32x4 v) { return __builtin_convertvector(v, bf16x4); }
DI bf16x8 pack8(f32x4 a, f32x4 b) { return __builtin_shufflevector(cvt4(a), cvt4(b), 0, 1, 2, 3, 4, 5, 6, 7); }
DI float xmax16_32(float m) {
  auto r = __builtin_amdgcn_permlane16_swap(__float_as_uint(m), __float_as_uint(m), false, false);
  m = fmaxf(__uint_as_float(r[0]), __uint_as_float(r[1]));
  auto r2 = __builtin_amdgcn_permlane32_swap(__float_as_uint(m), __float_as_uint(m), false, false);
  return fmaxf(__uint_as_float(r2[0]), __uint_as_float(r2[1]));
}
DI void st128_dev(bf16x8 v, srd_t srd, unsigned voff, unsigned soff) {
  asm volatile("buffer_store_dwordx4 %0, %1, %2, %3 offen sc1\n\ts_nop 1" :: "v"(v), "v"(voff), "s"(srd), "s"(soff) : "memory");
}
DI unsigned fresh_lane() { unsigned r; asm volatile("v_mbcnt_lo_u32_b32 %0, -1, 0\n\tv_mbcnt_hi_u32_b32 %0, -1, %0" : "=v"(r)); return r; }
DI unsigned ld_dev(const unsigned* p) { return __hip_atomic_load(p, __ATOMIC_RELAXED, __HIP_MEMORY_SCOPE_AGENT); }
DI void st_dev(unsigned* p, unsigned v) { __hip_atomic_store(p, v, __ATOMIC_RELAXED, __HIP_MEMORY_SCOPE_AGENT); }

#define STAMP(i)
#define STAMP_DECL
#define STAMP_OUT(base)
template <int I> struct IC { static constexpr int v = I; };
template <int I0, int I1, class F> DI void static_for(F&& f) {
  if constexpr (I0 < I1) { f(IC<I0>{}); static_for<I0 + 1, I1>(f); }
}
constexpr unsigned SPIN_CAP = 1u << 21;

constexpr int vslot(int t) { return t == 0 ? 0 : t + 1; }
constexpr int xwslot(int n) { return (n % 4) == 0 ? 4 : (n % 4); }
template <int G> constexpr int kslot(int t) { return G == 0 ? vslot(t) : (t == 0 ? 4 : t == 3 ? 0 : t + 1); }

template <int G>
DI void body(lds_char* L, const unsigned ldsb, const unsigned l, const unsigned w, const unsigned bid,
             const __bf16* __restrict__ x, const __bf16* __restrict__ kc, const __bf16* __restrict__ vc,
             const __bf16* __restrict__ wq, const __bf16* __restrict__ wo, char* __restrict__ ws, __bf16* __restrict__ out) {
  const unsigned xcd = bid & 7u, hh = (bid >> 3) & 3u, h = xcd * 4u + hh, b = bid >> 5;
  const srd_t srdX = __builtin_amdgcn_make_buffer_rsrc((void*)(x + (size_t)b * SQ * DM), (short)0, SQ * DM * 2, 0x00020000);
  const srd_t srdW = __builtin_amdgcn_make_buffer_rsrc((void*)(wq + h * DH), (short)0, (int)((DM * HD - h * DH) * 2), 0x00020000);
  const srd_t srdK = __builtin_amdgcn_make_buffer_rsrc((void*)(kc + (size_t)(b * NH + h) * SK * DH), (short)0, SK * DH * 2, 0x00020000);
  const srd_t srdV = __builtin_amdgcn_make_buffer_rsrc((void*)(vc + (size_t)(b * NH + h) * SK * DH), (short)0, SK * DH * 2, 0x00020000);
  const unsigned g = l >> 4, i16 = l & 15u;
  unsigned* sync = (unsigned*)(ws + WS_SYNC);
  unsigned hs0;
  {
    const unsigned* p = sync + bid * 64u + (l < 10u ? l : 32u);
    asm volatile("global_load_dword %0, %1, off sc1" : "=v"(hs0) : "v"(p) : "memory");
  }
  const unsigned xr0 = 12u * w + (l >> 3), xr1 = xr0 + 8u;
  const unsigned voffX0 = xr0 * (DM * 2) + (((l & 7u) ^ ((xr0 >> 1) & 7u)) << 4);
  const unsigned voffX1 = xr1 * (DM * 2) + (((l & 7u) ^ ((xr1 >> 1) & 7u)) << 4);
  const unsigned fW = 2u * (g | (((w >> 1) & 1u) << 2));
  const unsigned voffW = (4u * w + g) * (HD * 2) + ((i16 ^ fW) << 4);
  const unsigned voffK = (4u * w + g) * 256u + ((i16 ^ ((4u * w + g) & 15u)) << 4);
  const unsigned voffV = (4u * w + g) * 256u + ((i16 ^ (2u * ((4u * w + g) & 7u))) << 4);
  constexpr unsigned QIMG = SLOT, SCRM = SLOT + 24576u, SCRS = 2u * SLOT + 24576u;
  const unsigned rot = b + (hh >> 1) * 5u;
  auto ktile = [&](int n) { unsigned k = rot + (unsigned)n; k = k >= 20u ? k - 20u : k; return k >= 10u ? k - 10u : k; };
  auto xw_piece = [&](auto NI, auto PI) {
    constexpr int n = decltype(NI)::v, pc = decltype(PI)::v;
    constexpr unsigned slot = (unsigned)xwslot(n) * SLOT;
    const unsigned kt = ktile(n);
    if constexpr (pc == 0) dma16(ldsb + slot + w * 1024u, voffW, srdW, kt * 64u * (HD * 2));
    if constexpr (pc == 1) dma16(ldsb + slot + w * 1024u + 8192u, voffW, srdW, kt * 64u * (HD * 2) + 32u * (HD * 2));
    if constexpr (pc == 2) dma16(ldsb + slot + 16384u + w * 1536u, voffX0, srdX, kt * 128u);
    if constexpr (pc == 3) dma16lo(ldsb + slot + 16384u + w * 1536u + 1024u, voffX1, srdX, kt * 128u);
  };
  auto kv_piece = [&](auto PI) {
    constexpr int p = decltype(PI)::v, t = (p >> 2) & 3, j = p & 3;
    if constexpr (p < 16) dma16nt(ldsb + (unsigned)kslot<G>(t) * SLOT + j * 8192u + w * 1024u, voffK, srdK, (unsigned)t * SLOT + j * 8192u);
    else if constexpr (p < 32) dma16nt(ldsb + (unsigned)vslot(t) * SLOT + j * 8192u + w * 1024u, voffV, srdV, (unsigned)t * SLOT + j * 8192u);
  };
  STAMP_DECL STAMP(0);
  const unsigned qg = w >> 2, kq = w & 3u;
  const unsigned q4 = i16 >> 2, p4 = l & 3u;
  const unsigned krd = (32u * kq + i16) * 256u + ((g ^ i16) << 4);
  unsigned nonce = 0;
  auto make_nonce = [&]() {
    asm volatile("" : "+v"(hs0));
    if (w == 0) {
      const unsigned r0 = __builtin_amdgcn_readlane(hs0, 10);
      const unsigned cand = r0 + 1u + l;
      bool ok = true;
#pragma unroll
      for (int j = 0; j < 10; ++j) ok = ok && (cand != (unsigned)__builtin_amdgcn_readlane(hs0, j));
      const unsigned long long m = __ballot(ok);
      nonce = r0 + 1u + (unsigned)__builtin_ctzll(m);
      if (l == 0) st_dev(sync + bid * 64u + 32u, nonce);
    }
  };
  bf16x8 kf[4][8];
  if constexpr (G == 1) {
    static_for<0, 8>([&](auto PI) { kv_piece(PI); });
    static_for<0, 4>([&](auto TI) {
      constexpr int t = decltype(TI)::v;
      wait_bar<4>(); STAMP(13 + t);
      if constexpr (t == 0) make_nonce();
#pragma unroll
      for (int f = 0; f < 8; ++f) kf[t][f] = ld128(L, (unsigned)kslot<1>(t) * SLOT + (krd ^ ((f & 3) << 6)) + (f >> 2) * 4096u);
      if constexpr (t < 2) static_for<0, 4>([&](auto JI) { kv_piece(IC<4 * (t + 2) + decltype(JI)::v>{}); });
      else static_for<0, 4>([&](auto PI) { xw_piece(IC<t - 2>{}, PI); });
    });
    static_for<0, 4>([&](auto PI) { xw_piece(IC<2>{}, PI); });
  } else {
    static_for<0, 4>([&](auto PI) { xw_piece(IC<0>{}, PI); });
    static_for<0, 4>([&](auto PI) { xw_piece(IC<1>{}, PI); });
    static_for<0, 4>([&](auto PI) { xw_piece(IC<2>{}, PI); });
  }
  f32x4 qacc[2][3];
#pragma unroll
  for (int e = 0; e < 2; ++e)
#pragma unroll
    for (int j = 0; j < 3; ++j) qacc[e][j] = f32x4{0.f, 0.f, 0.f, 0.f};
  constexpr int P0 = G ? 16 : 0;
  {
    const unsigned fWr = 2u * (q4 | ((g & 1u) << 2));
    const unsigned wrd0 = (8u * g + q4) * 256u + (((4u * kq + (p4 >> 1)) ^ fWr) << 4) + 8u * (p4 & 1u);
    const unsigned wrd1 = wrd0 ^ 32u;
    const unsigned xrow = 48u * qg + i16;
    const unsigned xrd0 = 16384u + xrow * 128u + ((g ^ ((xrow >> 1) & 7u)) << 4);
    const unsigned xrd1 = xrd0 ^ 64u;
    bf16x8 a0[2], b0[3], a1[2], b1[3];
    auto stream = [&](auto CI) {
      constexpr int c = decltype(CI)::v;
      if constexpr ((c >> 2) <= 9) xw_piece(IC<(c >> 2)>{}, IC<(c & 3)>{});
      else if constexpr (c - 40 < 8) kv_piece(IC<P0 + c - 40>{});
    };
    auto step = [&](bf16x8 (&ca)[2], bf16x8 (&cb)[3], bf16x8 (&na)[2], bf16x8 (&nb)[3], auto SLOTI, auto KSI, auto RDI, auto C0I) {
      constexpr unsigned slot = (unsigned)decltype(SLOTI)::v * SLOT;
      constexpr int ks = decltype(KSI)::v, c0 = decltype(C0I)::v;
      constexpr bool RD = decltype(RDI)::v;
      const unsigned xr = slot + (ks ? xrd1 : xrd0);
      qacc[0][0] = mfma16(ca[0], cb[0], qacc[0][0]); SB();
      if constexpr (RD) na[0] = ldtr2(L, slot + wrd0 + ks * 8192u, slot + wrd0 + ks * 8192u + 1024u);
      SB();
      qacc[0][1] = mfma16(ca[0], cb[1], qacc[0][1]); SB();
      if constexpr (RD) nb[0] = ld128(L, xr);
      SB();
      qacc[0][2] = mfma16(ca[0], cb[2], qacc[0][2]); SB();
      if constexpr (RD) nb[1] = ld128(L, xr + 2048u);
      if constexpr (c0 >= 0) stream(IC<(c0 >= 0 ? c0 : 0)>{});
      SB();
      qacc[1][0] = mfma16(ca[1], cb[0], qacc[1][0]); SB();
      if constexpr (RD) nb[2] = ld128(L, xr + 4096u);
      SB();
      qacc[1][1] = mfma16(ca[1], cb[1], qacc[1][1]); SB();
      if constexpr (RD) na[1] = ldtr2(L, slot + wrd1 + ks * 8192u, slot + wrd1 + ks * 8192u + 1024u);
      SB();
      qacc[1][2] = mfma16(ca[1], cb[2], qacc[1][2]); SB();
      if constexpr (c0 >= 0) stream(IC<(c0 >= 0 ? c0 + 1 : 0)>{});
      SB();
    };
    wait_bar<8>(); STAMP(1);
    if constexpr (G == 0) make_nonce();
    constexpr unsigned s0 = (unsigned)xwslot(0) * SLOT;
    a0[0] = ldtr2(L, s0 + wrd0, s0 + wrd0 + 1024u);
#pragma unroll
    for (int j = 0; j < 3; ++j) b0[j] = ld128(L, s0 + xrd0 + j * 2048u);
    a0[1] = ldtr2(L, s0 + wrd1, s0 + wrd1 + 1024u);
    SB();
    static_for<0, 10>([&](auto NI) {
      constexpr int n = decltype(NI)::v;
      step(a0, b0, a1, b1, IC<xwslot(n)>{}, IC<1>{}, IC<1>{}, IC<(n >= 1 ? 4 * (n + 2) + 2 : -1)>{});
      if constexpr (n < 9) wait_vm_bar<4>(); else bar_only();
      if constexpr (n == 2 || n == 7) { STAMP(2 + n); }
      SB();
      step(a1, b1, a0, b0, IC<xwslot(n + 1)>{}, IC<0>{}, IC<(n < 9)>{}, IC<4 * (n + 3)>{});
    });
  }
#pragma unroll
  for (int e = 0; e < 2; ++e)
#pragma unroll
    for (int j = 0; j < 3; ++j) {
      const unsigned q = 48u * qg + 16u * j + i16, c = 2u * (2u * kq + e) + (g >> 1);
      *(lds_bf16x4*)(L + QIMG + q * 256u + ((c ^ i16) << 4) + 8u * (g & 1u)) = cvt4(qacc[e][j]);
    }
  STAMP(12);
  f32x4 s[4][2][3];
  const unsigned qrd = QIMG + (48u * qg + i16) * 256u + ((g ^ i16) << 4);
  float mx[3];
  auto tile_max = [&](auto TI) {
    constexpr int t = decltype(TI)::v;
#pragma unroll
    for (int j = 0; j < 3; ++j) {
      float m = fmaxf(fmaxf(s[t][0][j][0], s[t][0][j][1]), fmaxf(s[t][0][j][2], s[t][0][j][3]));
      m = fmaxf(m, fmaxf(fmaxf(s[t][1][j][0], s[t][1][j][1]), fmaxf(s[t][1][j][2], s[t][1][j][3])));
      mx[j] = t == 0 ? m : fmaxf(mx[j], m);
      asm volatile("" : "+v"(mx[j]));
    }
  };
  {
    bf16x8 qf[3][4];
    if constexpr (G == 0) {
      static_for<0, 4>([&](auto TI) {
        constexpr int t = decltype(TI)::v;
        constexpr unsigned slot = (unsigned)kslot<0>(t) * SLOT;
        if constexpr (t == 0) wait_bar<4>(); else wait_vm_bar<4>();
        STAMP(13 + t);
        bf16x8 kfr[8];
        kfr[0] = ld128(L, slot + krd);
        if constexpr (t == 0) {
#pragma unroll
          for (int j = 0; j < 3; ++j) qf[j][0] = ld128(L, qrd + j * 4096u);
        }
        kfr[1] = ld128(L, slot + (krd ^ 64u));
        if constexpr (t == 0) {
#pragma unroll
          for (int j = 0; j < 3; ++j) qf[j][1] = ld128(L, (qrd ^ 64u) + j * 4096u);
        }
        kv_piece(IC<4 * t + 8>{});
        SB();
        static_for<0, 8>([&](auto FI) {
          constexpr int f = decltype(FI)::v, kt = f >> 2, ks = f & 3;
          if constexpr (f + 2 < 8) kfr[f + 2] = ld128(L, slot + (krd ^ (((f + 2) & 3) << 6)) + ((f + 2) >> 2) * 4096u);
          if constexpr (t == 0 && f < 2) {
#pragma unroll
            for (int j = 0; j < 3; ++j) qf[j][f + 2] = ld128(L, (qrd ^ ((f + 2) << 6)) + j * 4096u);
          }
          SB();
#pragma unroll
          for (int j = 0; j < 3; ++j) s[t][kt][j] = mfma16(kfr[f], qf[j][ks], ks == 0 ? f32x4{0.f, 0.f, 0.f, 0.f} : s[t][kt][j]);
          if constexpr (f == 3) kv_piece(IC<4 * t + 9>{});
          SB();
        });
        tile_max(TI);
        SB();
        if constexpr (t < 3) { wait_vm<4>(); kv_piece(IC<4 * t + 10>{}); kv_piece(IC<4 * t + 11>{}); }
      });
    } else {
      bar_only();
#pragma unroll
      for (int ks = 0; ks < 4; ++ks)
#pragma unroll
        for (int j = 0; j < 3; ++j) qf[j][ks] = ld128(L, (qrd ^ (ks << 6)) + j * 4096u);
      SB();
      static_for<0, 4>([&](auto TI) {
        constexpr int t = decltype(TI)::v;
        static_for<0, 8>([&](auto FI) {
          constexpr int f = decltype(FI)::v, kt = f >> 2, ks = f & 3;
#pragma unroll
          for (int j = 0; j < 3; ++j) s[t][kt][j] = mfma16(kf[t][f], qf[j][ks], ks == 0 ? f32x4{0.f, 0.f, 0.f, 0.f} : s[t][kt][j]);
          if constexpr (t >= 1 && f == 1) tile_max(IC<(t >= 1 ? t - 1 : 0)>{});
          if constexpr (t >= 2 && f == 4) kv_piece(IC<24 + (t - 2)>{});
          SB();
        });
      });
      tile_max(IC<3>{});
    }
  }
#pragma unroll
  for (int j = 0; j < 3; ++j) {
    const float m = xmax16_32(mx[j]);
    if (g == 0) *(lds_f32*)(L + SCRM + (w * 48u + 16u * j + i16) * 4u) = m;
  }
  bar_only(); STAMP(17);
  constexpr float CS = 0.08838834764831845f * 1.4426950408889634f;
  const __bf16 one = (__bf16)1.0f;
  const bf16x8 ones = {one, one, one, one, one, one, one, one};
  float mrow[3], lsum[3];
#pragma unroll
  for (int j = 0; j < 3; ++j) {
    float m = *(lds_f32*)(L + SCRM + ((qg * 4u + 0u) * 48u + 16u * j + i16) * 4u);
#pragma unroll
    for (int u = 1; u < 4; ++u) m = fmaxf(m, *(lds_f32*)(L + SCRM + ((qg * 4u + u) * 48u + 16u * j + i16) * 4u));
    mrow[j] = -m * CS;
    lsum[j] = 0.f;
  }
  bf16x4 ph[4][3][2];
  auto exp_unit = [&](auto TI, auto UI) {
    constexpr int t = decltype(TI)::v, j = decltype(UI)::v >> 1, kt = decltype(UI)::v & 1;
    const f32x2 nm = {mrow[j], mrow[j]}, cs2 = {CS, CS};
    const f32x2 u0 = __builtin_elementwise_fma(f32x2{s[t][kt][j][0], s[t][kt][j][1]}, cs2, nm);
    const f32x2 u1 = __builtin_elementwise_fma(f32x2{s[t][kt][j][2], s[t][kt][j][3]}, cs2, nm);
    const f32x4 p = {__builtin_amdgcn_exp2f(u0[0]), __builtin_amdgcn_exp2f(u0[1]), __builtin_amdgcn_exp2f(u1[0]), __builtin_amdgcn_exp2f(u1[1])};
    ph[t][j][kt] = cvt4(p);
    asm volatile("" : "+v"(ph[t][j][kt]));
  };
  auto pfrag = [&](int t, int j) { return __builtin_shufflevector(ph[t][j][0], ph[t][j][1], 0, 1, 2, 3, 4, 5, 6, 7); };
  constexpr int PN = G ? 26 : 22;
  kv_piece(IC<PN>{});
  static_for<0, 6>([&](auto UI) { exp_unit(IC<0>{}, UI); });
  kv_piece(IC<PN + 1>{});
  SB();
  const srd_t srdWo = __builtin_amdgcn_make_buffer_rsrc((void*)(wo + (size_t)xcd * 512u * DM + hh * 160u), (short)0, (int)((512 * DM - hh * 160u) * 2), 0x00020000);
  auto wo_chunk = [&](int i) {
    const unsigned hd = ((unsigned)i + b) & 3u, lw = fresh_lane();
#pragma unroll
    for (int k = 0; k < 5; ++k) {
      const unsigned c = (unsigned)k * 64u + lw, row = c / 20u, cc = c - row * 20u;
      const unsigned voff = (16u * w + row) * (DM * 2) + ((cc ^ (((row >> 2) & 1u) << 1)) << 4);
      dma16(ldsb + (unsigned)i * 40960u + w * 5120u + k * 1024u, voff, srdWo, hd * 128u * (DM * 2));
    }
  };
  f32x4 o[8][3];
  const unsigned fV = 2u * (4u * (g & 1u) + q4);
  const unsigned vrk = ((32u * kq + 4u * g + q4) * 256u + (((p4 >> 1) ^ fV) << 4) + 8u * (p4 & 1u)) ^ (kq << 6);
  unsigned pn = 0;
  STAMP(18);
  static_for<0, 4>([&](auto TI) {
    constexpr int t = decltype(TI)::v;
    constexpr unsigned slot = (unsigned)vslot(t) * SLOT;
    constexpr int issued = G == 0 ? (t == 0 ? 24 : t == 1 ? 28 : t == 2 ? 32 : 37) : (t == 0 ? 28 : t == 1 ? 32 : 37);
    if constexpr (t == 3) wait_bar<issued - (20 + 4 * t)>(); else wait_vm_bar<issued - (20 + 4 * t)>();
    STAMP(19 + t);
    bf16x8 vf[8];
    auto rdv = [&](int v) { const int i = (v + 2) & 7; const unsigned a = slot + (vrk ^ (i << 5)); vf[v] = ldtr2(L, a, a + 4096u); };
    rdv(0); rdv(1);
    {
#pragma unroll
      for (int j = 0; j < 3; ++j) {
        const f32x4 ls = mfma16(ones, pfrag(t, j), f32x4{0.f, 0.f, 0.f, 0.f});
        if constexpr (t < 3) lsum[j] += ls[0];
        else { if (g == 0) *(lds_f32*)(L + SCRS + (w * 48u + 16u * j + i16) * 4u) = lsum[j] + ls[0]; }
      }
    }
    SB();
    static_for<0, 8>([&](auto VI) {
      constexpr int v = decltype(VI)::v, i = (v + 2) & 7;
      if constexpr (v + 2 < 8) rdv(v + 2);
      if constexpr (issued < 32 && (v == 0 || v == 4)) kv_piece(IC<issued + (v >> 2)>{});
      SB();
#pragma unroll
      for (int j = 0; j < 3; ++j) o[i][j] = mfma16(vf[v], pfrag(t, j), t == 0 ? f32x4{0.f, 0.f, 0.f, 0.f} : o[i][j]);
      if constexpr (t < 3 && v < 6) exp_unit(IC<(t < 3 ? t + 1 : 0)>{}, IC<(v < 6 ? v : 0)>{});
      SB();
      if constexpr (t == 3 && v >= 3 && (v & 1)) {
        constexpr int pr = (v - 3) / 2 + 1;
        const unsigned rbase = (pr == 1 ? SLOT + 8192u : pr == 2 ? 2u * SLOT : 3u * SLOT) + (qg * 4u + (kq ^ (unsigned)pr)) * 3072u + l * 16u;
#pragma unroll
        for (int j = 0; j < 3; ++j) *(lds_bf16x8*)(L + rbase + j * 1024u) = pack8(o[2 * pr][j], o[2 * pr + 1][j]);
        SB();
      }
    });
    if constexpr (issued < 32) { wait_vm<4>(); kv_piece(IC<issued + 2>{}); kv_piece(IC<issued + 3>{}); }
    if constexpr (issued == 32) wo_chunk(0);
  });
  bar_only(); STAMP(23);
  const unsigned lt = fresh_lane(), gt = lt >> 4, it = lt & 15u;
  const unsigned pj = lt < 10u ? lt : 0u;
  const unsigned pbid = pj < 3u ? (bid ^ ((pj + 1u) << 3)) : (bid ^ (pj - 2u));
  const srd_t srdWS = __builtin_amdgcn_make_buffer_rsrc((void*)ws, (short)0, (int)WS_END, 0x00020000);
  {
    bf16x8 rr[3][3];
#pragma unroll
    for (int pr = 1; pr < 4; ++pr) {
      const unsigned rbase = (pr == 1 ? SLOT + 8192u : pr == 2 ? 2u * SLOT : 3u * SLOT) + w * 3072u + lt * 16u;
#pragma unroll
      for (int j = 0; j < 3; ++j) rr[pr - 1][j] = ld128(L, rbase + j * 1024u);
    }
    float inv[3];
#pragma unroll
    for (int j = 0; j < 3; ++j) {
      float sum = 0.f;
#pragma unroll
      for (int u = 0; u < 4; ++u) sum += *(lds_f32*)(L + SCRS + ((qg * 4u + u) * 48u + 16u * j + it) * 4u);
      inv[j] = __builtin_amdgcn_rcpf(sum);
    }
    bf16x8 sel0, sel1;
#pragma unroll
    for (int jj = 0; jj < 4; ++jj) {
      const __bf16 vv = (4u * gt + jj == it) ? one : (__bf16)0.0f;
      sel0[jj] = vv; sel0[4 + jj] = (__bf16)0.0f; sel1[jj] = (__bf16)0.0f; sel1[4 + jj] = vv;
    }
#pragma unroll
    for (int pr = 0; pr < 3; ++pr)
#pragma unroll
      for (int j = 0; j < 3; ++j) { o[0][j] = mfma16(sel0, rr[pr][j], o[0][j]); o[1][j] = mfma16(sel1, rr[pr][j], o[1][j]); }
#pragma unroll
    for (int j = 0; j < 3; ++j) {
      const bf16x8 pk = pack8(o[0][j] * inv[j], o[1][j] * inv[j]);
      st128_dev(pk, srdWS, lt * 16u, WS_O + bid * 24576u + ((3u * qg + j) * 4u + kq) * 1024u);
    }
  }
  bar_only(); STAMP(24);
  wo_chunk(1); wo_chunk(2); wo_chunk(3);
  if (w == 0) {
    const unsigned* p = sync + pbid * 64u + 32u;
    asm volatile("global_load_dword %0, %1, off sc1\n\ts_waitcnt vmcnt(0)" : "=&v"(pn) : "v"(p) : "memory");
  } else {
    wait_vm<15>();
  }
  asm volatile("s_barrier" ::: "memory");
  STAMP(25);
  auto exchange = [&](unsigned lo, unsigned hi) {
    if (w == 0) {
      const bool act = lt >= lo && lt < hi;
      if (act) st_dev(sync + pbid * 64u + pj, pn);
      for (unsigned spin = 0; spin < SPIN_CAP; ++spin) {
        const unsigned e = ld_dev(sync + bid * 64u + pj);
        if (__all(!act || e == nonce)) break;
        if ((spin & 63u) == 63u) {
          const unsigned r = ld_dev(sync + pbid * 64u + 32u);
          if (act && r != pn) st_dev(sync + pbid * 64u + pj, r);
          if (act) pn = r;
        }
        __builtin_amdgcn_s_sleep(1);
      }
    }
  };
  auto recheck = [&](unsigned lo, unsigned hi) {
    if (w == 0) {
      const bool act = lt >= lo && lt < hi;
      const unsigned r = ld_dev(sync + pbid * 64u + 32u);
      if (act && r != pn) st_dev(sync + pbid * 64u + pj, r);
      pn = r;
    }
  };
  exchange(0u, 3u);
  asm volatile("s_waitcnt vmcnt(0)\n\ts_barrier" ::: "memory");
  STAMP(26);
  const unsigned qh = w >> 2, nh = (w >> 1) & 1u, kp = w & 1u;
  f32x4 acc[5][3];
  {
    u32x4 of[4][2][3];
#pragma unroll
    for (int i = 0; i < 4; ++i) {
      const unsigned src = (bid & ~24u) | ((((unsigned)i + b) & 3u) << 3);
#pragma unroll
      for (int kk = 0; kk < 2; ++kk)
#pragma unroll
        for (int j = 0; j < 3; ++j)
          of[i][kk][j] = __builtin_amdgcn_raw_buffer_load_b128(srdWS, lt * 16u, WS_O + src * 24576u + ((3u * qh + j) * 4u + kp + 2u * kk) * 1024u, 16);
    }
    const unsigned wrow = 32u * kp + 4u * gt + (it >> 2);
    const unsigned wrd = wrow * 320u + nh * 160u + 8u * ((lt & 3u) & 1u);
    const unsigned wsw = (gt & 1u) << 1;
    unsigned wa[5];
#pragma unroll
    for (int nt = 0; nt < 5; ++nt) {
      const unsigned cc = nh * 10u + 2u * nt + ((lt & 3u) >> 1);
      wa[nt] = wrow * 320u + ((cc ^ wsw) << 4) + 8u * ((lt & 3u) & 1u);
    }
    (void)wrd;
    SB();
    bf16x8 af[2][5];
#pragma unroll
    for (int nt = 0; nt < 5; ++nt) af[0][nt] = ldtr2(L, wa[nt], wa[nt] + 5120u);
    static_for<0, 8>([&](auto SI) {
      constexpr int st = decltype(SI)::v, i = st >> 1, kk = st & 1, cur = st & 1;
      constexpr unsigned nb = (unsigned)((st + 1) >> 1) * 40960u + (unsigned)((st + 1) & 1) * 20480u;
      static_for<0, 5>([&](auto NTI) {
        constexpr int nt = decltype(NTI)::v;
        if constexpr (st < 7) af[cur ^ 1][nt] = ldtr2(L, nb + wa[nt], nb + wa[nt] + 5120u);
        SB();
#pragma unroll
        for (int j = 0; j < 3; ++j)
          acc[nt][j] = mfma16(af[cur][nt], __builtin_bit_cast(bf16x8, of[i][kk][j]), st == 0 ? f32x4{0.f, 0.f, 0.f, 0.f} : acc[nt][j]);
        SB();
      });
    });
  }
  STAMP(27);
  recheck(0u, 3u);
  bar_only();
  {
    const unsigned xb = (w ^ 1u) * 8192u + lt * 16u, rb = w * 8192u + lt * 16u;
    if (kp == 0) {
#pragma unroll
      for (int f = 8; f < 15; ++f) *(lds_f32x4*)(L + xb + (f - 8) * 1024u) = acc[f / 3][f % 3];
    } else {
#pragma unroll
      for (int f = 0; f < 8; ++f) *(lds_f32x4*)(L + xb + f * 1024u) = acc[f / 3][f % 3];
    }
    bar_only();
    const unsigned owner = (bid & ~7u) | (lt >> 3);
    const unsigned vo = WS_P + owner * 32768u + xcd * 4096u + (lt & 7u) * 16u;
    if (kp == 0) {
#pragma unroll
      for (int f = 0; f < 8; ++f) acc[f / 3][f % 3] += *(lds_f32x4*)(L + rb + f * 1024u);
#pragma unroll
      for (int u = 0; u < 4; ++u) {
        const bf16x8 pk = pack8(acc[(2 * u) / 3][(2 * u) % 3], acc[(2 * u + 1) / 3][(2 * u + 1) % 3]);
        st128_dev(pk, srdWS, vo, ((w >> 1) * 8u + u) * 128u);
      }
    } else {
#pragma unroll
      for (int f = 8; f < 15; ++f) acc[f / 3][f % 3] += *(lds_f32x4*)(L + rb + (f - 8) * 1024u);
#pragma unroll
      for (int u = 4; u < 8; ++u) {
        const bf16x8 pk = pack8(acc[(2 * u) / 3][(2 * u) % 3], u < 7 ? acc[(2 * u + 1) / 3 % 5][(2 * u + 1) % 3] : f32x4{0.f, 0.f, 0.f, 0.f});
        st128_dev(pk, srdWS, vo, ((w >> 1) * 8u + u) * 128u);
      }
    }
  }
  asm volatile("s_waitcnt vmcnt(0)\n\ts_barrier" ::: "memory");
  STAMP(28);
  exchange(3u, 10u);
  asm volatile("s_barrier" ::: "memory");
  STAMP(29);
  if (w < 4) {
    const unsigned c = w * 64u + lt;
    u32x4 r[8];
#pragma unroll
    for (int i = 0; i < 8; ++i) r[i] = __builtin_amdgcn_raw_buffer_load_b128(srdWS, c * 16u, WS_P + bid * 32768u + i * 4096u, 16);
    recheck(3u, 10u);
    f32x4 f0 = {0.f, 0.f, 0.f, 0.f}, f1 = f0;
#pragma unroll
    for (int i = 0; i < 8; ++i) {
      const bf16x8 v = __builtin_bit_cast(bf16x8, r[i]);
#pragma unroll
      for (int q = 0; q < 4; ++q) { f0[q] += (float)v[q]; f1[q] += (float)v[4 + q]; }
    }
    const unsigned pw = c >> 6, u = (c >> 3) & 7u, sl = 8u * xcd + (c & 7u);
    const unsigned fa = 2u * u, fb = 2u * u + 1u;
    const unsigned qa = 48u * (pw >> 1) + 16u * (fa % 3u) + (sl & 15u), na = 160u * hh + 80u * (pw & 1u) + 16u * (fa / 3u) + 4u * (sl >> 4);
    const unsigned qb = 48u * (pw >> 1) + 16u * (fb % 3u) + (sl & 15u), nb2 = 160u * hh + 80u * (pw & 1u) + 16u * (fb / 3u) + 4u * (sl >> 4);
    *(bf16x4*)(out + (size_t)(b * SQ + qa) * DM + na) = cvt4(f0);
    if (u < 7u) *(bf16x4*)(out + (size_t)(b * SQ + qb) * DM + nb2) = cvt4(f1);
  }
  STAMP(30);
  STAMP_OUT(ws + (64u << 20));
}

__global__ __launch_bounds__(512, 2)
void gqa_fused(const __bf16* __restrict__ x, const __bf16* __restrict__ kc, const __bf16* __restrict__ vc,
               const __bf16* __restrict__ wq, const __bf16* __restrict__ wo, char* __restrict__ ws, __bf16* __restrict__ out) {
  extern __shared__ __attribute__((aligned(16))) char smem[];
  lds_char* L = (lds_char*)smem;
  const unsigned ldsb = (unsigned)(size_t)L;
  const unsigned tid = threadIdx.x, l = tid & 63u;
  unsigned w = __builtin_amdgcn_readfirstlane(tid >> 6);
  asm volatile("s_nop 4" : "+s"(w));
  const unsigned bid = blockIdx.x;
#define GSEL ((bid >> 3) & 1u)
  if (GSEL) body<1>(L, ldsb, l, w, bid, x, kc, vc, wq, wo, ws, out);
  else body<0>(L, ldsb, l, w, bid, x, kc, vc, wq, wo, ws, out);
}

extern "C" void kernel_launch(void* const* d_in, const int* in_sizes, int n_in, void* d_out, int out_size, void* d_ws,
                              size_t ws_size, hipStream_t stream) {
  (void)hipFuncSetAttribute((const void*)gqa_fused, hipFuncAttributeMaxDynamicSharedMemorySize, LDS_BYTES);
  hipLaunchKernelGGL(gqa_fused, dim3(256), dim3(512), LDS_BYTES, stream, (const __bf16*)d_in[0], (const __bf16*)d_in[1],
                     (const __bf16*)d_in[2], (const __bf16*)d_in[3], (const __bf16*)d_in[4], (char*)d_ws, (__bf16*)d_out);
}
```

```cpp
#include <hip/hip_runtime.h>
#include <hip/hip_bf16.h>

#define DI __device__ __forceinline__
typedef __attribute__((ext_vector_type(8))) __bf16 bf16x8;
typedef __attribute__((ext_vector_type(4))) __bf16 bf16x4;
typedef __attribute__((ext_vector_type(2))) __bf16 bf16x2;
typedef __attribute__((ext_vector_type(4))) float f32x4;
typedef __attribute__((ext_vector_type(2))) float f32x2;
typedef __attribute__((ext_vector_type(4))) unsigned u32x4;
typedef __attribute__((address_space(3))) char lds_char;
typedef __attribute__((address_space(3))) bf16x8 lds_bf16x8;
typedef __attribute__((address_space(3))) bf16x4 lds_bf16x4;
typedef __attribute__((address_space(3))) f32x4 lds_f32x4;
typedef __attribute__((address_space(3))) float lds_f32;
typedef __amdgpu_buffer_rsrc_t srd_t;

constexpr int NB = 8, SQ = 96, DM = 640, NH = 32, SK = 512, DH = 128;
constexpr int HD = NH * DH;
constexpr unsigned SLOT = 32768;
constexpr unsigned LDS_BYTES = 5 * SLOT;
constexpr unsigned WS_SYNC = 0;
constexpr unsigned WS_O = 65536;
constexpr unsigned WS_P = WS_O + 256u * 24576u;
constexpr unsigned WS_END = WS_P + 256u * 32768u;

#define SB() __builtin_amdgcn_sched_barrier(0)
DI void dma16(unsigned m0v, unsigned voff, srd_t srd, unsigned soff) {
  asm volatile("s_mov_b32 m0, %0\n\ts_nop 0\n\tbuffer_load_dwordx4 %1, %2, %3 offen lds"
               :: "s"(m0v), "v"(voff), "s"(srd), "s"(soff) : "memory");
}
DI void dma16nt(unsigned m0v, unsigned voff, srd_t srd, unsigned soff) {
  asm volatile("s_mov_b32 m0, %0\n\ts_nop 0\n\tbuffer_load_dwordx4 %1, %2, %3 offen nt lds"
               :: "s"(m0v), "v"(voff), "s"(srd), "s"(soff) : "memory");
}
template <int N> DI void wait_vm_bar() { asm volatile("s_waitcnt vmcnt(%0)\n\ts_barrier" :: "i"(N) : "memory"); }
DI void bar_only() { asm volatile("s_waitcnt lgkmcnt(0)\n\ts_barrier" ::: "memory"); }
DI f32x4 mfma16(bf16x8 a, bf16x8 b, f32x4 c) { return __builtin_amdgcn_mfma_f32_16x16x32_bf16(a, b, c, 0, 0, 0); }
DI bf16x8 ld128(lds_char* L, unsigned off) { return *(lds_bf16x8*)(L + off); }
DI bf16x8 ldtr2(lds_char* L, unsigned off0, unsigned off1) {
  bf16x4 a = __builtin_amdgcn_ds_read_tr16_b64_v4bf16((lds_bf16x4*)(L + off0));
  bf16x4 b = __builtin_amdgcn_ds_read_tr16_b64_v4bf16((lds_bf16x4*)(L + off1));
  return __builtin_shufflevector(a, b, 0, 1, 2, 3, 4, 5, 6, 7);
}
DI bf16x4 cvt4(f32x4 v) { return __builtin_convertvector(v, bf16x4); }
DI bf16x8 pack8(f32x4 a, f32x4 b) { return __builtin_shufflevector(cvt4(a), cvt4(b), 0, 1, 2, 3, 4, 5, 6, 7); }
DI float xmax16_32(float m) {
  auto r = __builtin_amdgcn_permlane16_swap(__float_as_uint(m), __float_as_uint(m), false, false);
  m = fmaxf(__uint_as_float(r[0]), __uint_as_float(r[1]));
  auto r2 = __builtin_amdgcn_permlane32_swap(__float_as_uint(m), __float_as_uint(m), false, false);
  return fmaxf(__uint_as_float(r2[0]), __uint_as_float(r2[1]));
}
DI void st128_dev(bf16x8 v, srd_t srd, unsigned voff, unsigned soff) {
  asm volatile("buffer_store_dwordx4 %0, %1, %2, %3 offen sc1\n\ts_nop 1" :: "v"(v), "v"(voff), "s"(srd), "s"(soff) : "memory");
}
DI unsigned fresh_lane() { unsigned r; asm volatile("v_mbcnt_lo_u32_b32 %0, -1, 0\n\tv_mbcnt_hi_u32_b32 %0, -1, %0" : "=v"(r)); return r; }
DI float xsum16_32(float m) {
  auto r = __builtin_amdgcn_permlane16_swap(__float_as_uint(m), __float_as_uint(m), false, false);
  m = __uint_as_float(r[0]) + __uint_as_float(r[1]);
  auto r2 = __builtin_amdgcn_permlane32_swap(__float_as_uint(m), __float_as_uint(m), false, false);
  return __uint_as_float(r2[0]) + __uint_as_float(r2[1]);
}
DI unsigned ld_dev(const unsigned* p) { return __hip_atomic_load(p, __ATOMIC_RELAXED, __HIP_MEMORY_SCOPE_AGENT); }
DI void st_dev(unsigned* p, unsigned v) { __hip_atomic_store(p, v, __ATOMIC_RELAXED, __HIP_MEMORY_SCOPE_AGENT); }

template <int I> struct IC { static constexpr int v = I; };
template <int I0, int I1, class F> DI void static_for(F&& f) {
  if constexpr (I0 < I1) { f(IC<I0>{}); static_for<I0 + 1, I1>(f); }
}
constexpr unsigned SPIN_CAP = 1u << 21;
#define ROTH ((hh * 5u + 1u) >> 1)

constexpr int kvslot(int n) { return (n + 1) % 3; }
constexpr int xwslot(int n) { return (n % 5) == 0 ? 1 : (n % 5) == 1 ? 2 : (n % 5) == 2 ? 0 : (n % 5); }
constexpr unsigned QIMG = 98304u, PB1 = 98304u, PB0 = 122880u, SCRM = 147456u, SCRS = 151552u, SCRF = 153600u, OIMG = 40960u;
#define THR 40.0f
DI unsigned opq(unsigned v) { asm volatile("" : "+v"(v)); return v; }
DI void bar() { asm volatile("s_barrier" ::: "memory"); }

DI void loader(lds_char* L, const unsigned ldsb, const unsigned l, const unsigned hw, const unsigned bid,
               const __bf16* __restrict__ x, const __bf16* __restrict__ kc, const __bf16* __restrict__ vc,
               const __bf16* __restrict__ wq, const __bf16* __restrict__ wo, char* __restrict__ ws) {
  const unsigned xcd = bid & 7u, hh = (bid >> 3) & 3u, h = xcd * 4u + hh, b = bid >> 5;
  const srd_t srdX = __builtin_amdgcn_make_buffer_rsrc((void*)(x + (size_t)b * SQ * DM), (short)0, SQ * DM * 2, 0x00020000);
  const srd_t srdW = __builtin_amdgcn_make_buffer_rsrc((void*)(wq + h * DH), (short)0, (int)((DM * HD - h * DH) * 2), 0x00020000);
  const srd_t srdK = __builtin_amdgcn_make_buffer_rsrc((void*)(kc + (size_t)(b * NH + h) * SK * DH), (short)0, SK * DH * 2, 0x00020000);
  const srd_t srdV = __builtin_amdgcn_make_buffer_rsrc((void*)(vc + (size_t)(b * NH + h) * SK * DH), (short)0, SK * DH * 2, 0x00020000);
  const srd_t srdWo = __builtin_amdgcn_make_buffer_rsrc((void*)(wo + (size_t)xcd * 512u * DM + hh * 160u), (short)0, (int)((512 * DM - hh * 160u) * 2), 0x00020000);
  const unsigned g = l >> 4, i16 = l & 15u, r4 = 4u * hw + g;
  const unsigned voffW = r4 * (HD * 2) + ((i16 ^ (2u * (g | (((hw >> 1) & 1u) << 2)))) << 4);
  const unsigned voffX = (8u * hw + (l >> 3)) * (DM * 2) + (((l & 7u) ^ (r4 & 7u)) << 4);
  const unsigned voffK = r4 * 256u + ((i16 ^ r4) << 4);
  const unsigned voffV = r4 * 256u + ((i16 ^ (2u * (r4 & 7u))) << 4);
  const unsigned rot = b + ROTH;
  auto xw_item = [&](int n) {
    unsigned kt = rot + (unsigned)n; kt = kt >= 20u ? kt - 20u : kt; kt = kt >= 10u ? kt - 10u : kt;
    const unsigned slot = ldsb + (unsigned)xwslot(n) * SLOT + hw * 1024u;
#pragma unroll
    for (int i = 0; i < 4; ++i) dma16(slot + i * 4096u, voffW, srdW, (kt * 64u + 16u * i) * (HD * 2));
#pragma unroll
    for (int i = 0; i < 3; ++i) dma16(slot + 16384u + i * 4096u, voffX, srdX, kt * 128u + 32u * i * (DM * 2));
  };
  auto kv_tile = [&](int n, int i0 = 0, int i1 = 8) {
    const unsigned slot = ldsb + (unsigned)kvslot(n) * SLOT + hw * 1024u;
#pragma unroll
    for (int i = i0; i < i1; ++i) {
      if (n < 4) dma16nt(slot + i * 4096u, voffK, srdK, (unsigned)n * SLOT + i * 4096u);
      else dma16nt(slot + i * 4096u, voffV, srdV, (unsigned)(n - 4) * SLOT + i * 4096u);
    }
  };
  unsigned wov[5];
  {
    unsigned row = (l * 205u) >> 12, cc = l - row * 20u;
#pragma unroll
    for (int k = 0; k < 5; ++k) {
      wov[k] = (16u * hw + row) * (DM * 2) + ((cc ^ (((row >> 2) & 1u) << 1)) << 4);
      const bool wrap = cc >= 16u;
      row += wrap ? 4u : 3u; cc += wrap ? (unsigned)-16 : 4u;
    }
  }
  auto wo_chunk = [&](int i) {
#pragma unroll
    for (int u = 0; u < 2; ++u)
#pragma unroll
      for (int k = 0; k < 5; ++k)
        dma16(ldsb + (unsigned)i * 40960u + (hw + 4u * u) * 5120u + k * 1024u, wov[k], srdWo, (((hh + (unsigned)i) & 3u) * 128u + 64u * u) * (DM * 2));
  };
  unsigned* sync = (unsigned*)(ws + WS_SYNC);
  const unsigned pj = l < 10u ? l : 0u;
  const unsigned pbid = pj < 3u ? (bid ^ ((pj + 1u) << 3)) : (bid ^ (pj - 2u));
  unsigned hs0;
  asm volatile("global_load_dword %0, %1, off sc1" : "=&v"(hs0) : "v"(sync + bid * 64u + (l < 10u ? l : 32u)) : "memory");
  xw_item(0); xw_item(1);
  wait_vm_bar<7>();
  xw_item(2); xw_item(3); xw_item(4);
#pragma unroll
  for (int n = 0; n < 9; ++n) {
    if (n < 6) wait_vm_bar<21>(); else if (n == 6) wait_vm_bar<22>(); else if (n == 7) wait_vm_bar<15>(); else wait_vm_bar<12>();
    if (n + 5 <= 9) xw_item(n + 5); else if (n == 5) kv_tile(0); else if (n == 7) kv_tile(1, 0, 4);
  }
  bar();
  kv_tile(1, 4, 8);
  asm volatile("" : "+v"(hs0));
  unsigned nonce;
  {
    const unsigned r0 = __builtin_amdgcn_readlane(hs0, 10);
    const unsigned d = hs0 - (r0 + 1u);
    const unsigned bit = d < 16u ? (1u << d) : 0u;
    unsigned m = 0;
#pragma unroll
    for (int j = 0; j < 10; ++j) m |= (unsigned)__builtin_amdgcn_readlane(bit, j);
    nonce = r0 + 1u + (unsigned)__builtin_ctz(~m);
  }
  wait_vm_bar<8>();
  kv_tile(2);
  {
    const unsigned* p = sync + bid * 64u + (hw == 0 ? 32u : 40u + hw);
    asm volatile("s_mov_b64 exec, 1\n\tglobal_store_dword %0, %1, off sc1\n\ts_mov_b64 exec, -1" :: "v"(p), "v"(nonce) : "memory");
  }
#pragma unroll
  for (int n = 1; n < 4; ++n) {
    wait_vm_bar<8>();
    kv_tile(n + 2);
  }
  wait_vm_bar<8>();
  if (__builtin_amdgcn_readfirstlane(__float_as_uint(*(lds_f32*)(L + SCRF))) != 0u) { bar(); bar(); }
  kv_tile(6);
  wait_vm_bar<8>(); kv_tile(7);
  unsigned pn;
  asm volatile("global_load_dword %0, %1, off sc1" : "=&v"(pn) : "v"(sync + pbid * 64u + 32u) : "memory");
  wait_vm_bar<9>();
  bar();
  wo_chunk(0);
  wait_vm_bar<11>();
  wo_chunk(3);
  wait_vm_bar<10>();
  asm volatile("" : "+v"(pn));
  wo_chunk(2);
  wait_vm_bar<0>();
  auto exchange = [&](unsigned lo, unsigned hi, bool dma) {
    const bool act = l >= lo && l < hi;
    if (act) st_dev(sync + pbid * 64u + pj, pn);
    if (dma) wo_chunk(1);
    for (unsigned spin = 0; spin < SPIN_CAP; ++spin) {
      const unsigned e = ld_dev(sync + bid * 64u + pj);
      if (__all(!act || e == nonce)) break;
      if ((spin & 63u) == 63u) {
        const unsigned r = ld_dev(sync + pbid * 64u + 32u);
        if (act && r != pn) st_dev(sync + pbid * 64u + pj, r);
        if (act) pn = r;
      }
      __builtin_amdgcn_s_sleep(1);
    }
  };
  auto recheck = [&](unsigned lo, unsigned hi) {
    const unsigned r = ld_dev(sync + pbid * 64u + 32u);
    if (l >= lo && l < hi && r != pn) st_dev(sync + pbid * 64u + pj, r);
    pn = r;
  };
  if (hw == 0) exchange(0u, 3u, true); else wo_chunk(1);
  wait_vm_bar<0>();
  if (hw == 0) recheck(0u, 3u);
  bar();
  bar();
  if (hw == 0) exchange(3u, 10u, false);
  bar();
  if (hw == 0) recheck(3u, 10u);
}

DI void compute(lds_char* L, const unsigned l, const unsigned w, const unsigned bid, const __bf16* __restrict__ wo,
                char* __restrict__ ws, __bf16* __restrict__ out, unsigned cpf) {
  const unsigned xcd = bid & 7u, hh = (bid >> 3) & 3u, b = bid >> 5;
  const unsigned g = l >> 4, i16 = l & 15u;
  const unsigned qg = w >> 2, kq = w & 3u;
  const unsigned q4 = i16 >> 2, p4 = l & 3u;
#define MM(ACC, A, B, ZERO, NEXT, G) { ACC = mfma16(A, B, ZERO ? f32x4{0.f, 0.f, 0.f, 0.f} : ACC); SB(); NEXT(IC<G>{}); SB(); }
#define STEP6(ACC, CA, CB, ZERO, NEXT)                                                              \
  MM(ACC[0][0], CA[0], CB[0], ZERO, NEXT, 0) MM(ACC[0][1], CA[0], CB[1], ZERO, NEXT, 1) MM(ACC[0][2], CA[0], CB[2], ZERO, NEXT, 2) \
  MM(ACC[1][0], CA[1], CB[0], ZERO, NEXT, 3) MM(ACC[1][1], CA[1], CB[1], ZERO, NEXT, 4) MM(ACC[1][2], CA[1], CB[2], ZERO, NEXT, 5) SB();
  {
    f32x4 qacc[2][3];
    const unsigned fWr = 2u * (q4 | ((g & 1u) << 2));
    const unsigned wrd0 = (8u * g + q4) * 256u + (((4u * kq + (p4 >> 1)) ^ fWr) << 4) + 8u * (p4 & 1u);
    const unsigned wrd1 = wrd0 ^ 32u;
    const unsigned xrow = 48u * qg + i16;
    const unsigned xrd0 = 16384u + xrow * 128u + ((g ^ ((xrow >> 1) & 7u)) << 4);
    const unsigned xrd1 = xrd0 ^ 64u;
    bf16x8 a0[2], b0[3], a1[2], b1[3];
    auto fill = [&](bf16x8 (&na)[2], bf16x8 (&nb)[3], auto SLOTI, auto KSI, auto RDI, auto GI) {
      constexpr unsigned slot = (unsigned)decltype(SLOTI)::v * SLOT;
      constexpr int ks = decltype(KSI)::v, gap = decltype(GI)::v;
      const unsigned xr = slot + (ks ? xrd1 : xrd0);
      if constexpr (decltype(RDI)::v) {
        if constexpr (gap == 0) na[0] = ldtr2(L, slot + wrd0 + ks * 8192u, slot + wrd0 + ks * 8192u + 1024u);
        if constexpr (gap >= 1 && gap <= 3) nb[gap - 1] = ld128(L, xr + (gap - 1) * 2048u);
        if constexpr (gap == 4) na[1] = ldtr2(L, slot + wrd1 + ks * 8192u, slot + wrd1 + ks * 8192u + 1024u);
      }
    };
    bar();
    u32x4 tch;
    {
      const srd_t srdWo = __builtin_amdgcn_make_buffer_rsrc((void*)(wo + (size_t)xcd * 512u * DM + hh * 160u), (short)0, (int)((512 * DM - hh * 160u) * 2), 0x00020000);
      const unsigned vo = (l >> 3) * (DM * 2) + (l & 7u) * 16u, so = (64u * b + 8u * w) * (DM * 2);
      asm volatile("buffer_load_dwordx4 %0, %1, %2, %3 offen\n\tbuffer_load_dwordx4 %0, %1, %2, %3 offen offset:128\n\tbuffer_load_dwordx4 %0, %1, %2, %3 offen offset:192"
                   : "=&v"(tch) : "v"(vo), "s"(srdWo), "s"(so) : "memory");
    }
    static_for<0, 5>([&](auto GI) { fill(a0, b0, IC<xwslot(0)>{}, IC<0>{}, IC<1>{}, GI); });
    SB();
    static_for<0, 10>([&](auto NI) {
      constexpr int n = decltype(NI)::v;
      auto nxa = [&](auto GI) { fill(a1, b1, IC<xwslot(n)>{}, IC<1>{}, IC<1>{}, GI); };
      STEP6(qacc, a0, b0, (n == 0), nxa)
      bar_only();
      SB();
      auto nxb = [&](auto GI) { fill(a0, b0, IC<xwslot(n + 1)>{}, IC<0>{}, IC<(n < 9)>{}, GI); };
      STEP6(qacc, a1, b1, false, nxb)
    });
    asm volatile("s_waitcnt vmcnt(0)" :: "v"(tch), "v"(cpf) : "memory");
#pragma unroll
    for (int e = 0; e < 2; ++e)
#pragma unroll
      for (int j = 0; j < 3; ++j) {
        const unsigned q = 48u * qg + 16u * j + i16, c = 2u * (2u * kq + e) + (g >> 1);
        *(lds_bf16x4*)(L + QIMG + q * 256u + ((c ^ i16) << 4) + 8u * (g & 1u)) = cvt4(qacc[e][j]);
      }
  }
  constexpr float CS = 0.08838834764831845f * 1.4426950408889634f;
  const __bf16 one = (__bf16)1.0f;
  f32x4 s[2][2][3];
  float mrow[3];
  f32x2 lsum[3] = {{0.f, 0.f}, {0.f, 0.f}, {0.f, 0.f}};
  unsigned pr[4][3][4];
  const unsigned scw = opq(SCRM + (w * 48u + i16) * 4u), scr = opq(SCRM + (qg * 192u + i16) * 4u);
  auto lane_max = [&](int par, int j) {
    const float m0 = fmaxf(fmaxf(s[par][0][j][0], s[par][0][j][1]), s[par][0][j][2]);
    const float m1 = fmaxf(fmaxf(s[par][0][j][3], s[par][1][j][0]), s[par][1][j][1]);
    return fmaxf(fmaxf(m0, m1), fmaxf(s[par][1][j][2], s[par][1][j][3]));
  };
  auto rescale = [&](auto NTI, const float (&nm)[3]) {
#pragma unroll
    for (int j = 0; j < 3; ++j) {
      const float f = __builtin_amdgcn_exp2f(nm[j] - mrow[j]);
      mrow[j] = nm[j];
      lsum[j] *= f;
#pragma unroll
      for (int tt = 0; tt < decltype(NTI)::v; ++tt)
#pragma unroll
        for (int d = 0; d < 4; ++d) {
          const f32x2 v = {__uint_as_float(pr[tt][j][d] << 16) * f, __uint_as_float(pr[tt][j][d] & 0xffff0000u) * f};
          pr[tt][j][d] = __builtin_bit_cast(unsigned, __builtin_convertvector(v, bf16x2));
        }
    }
  };
  auto check = [&](auto TI) {
    constexpr int t = decltype(TI)::v;
    float m[3];
#pragma unroll
    for (int j = 0; j < 3; ++j) {
      m[j] = fmaxf(fmaxf(s[t & 1][0][j][0], s[t & 1][0][j][1]), s[t & 1][0][j][2]);
      m[j] = fmaxf(fmaxf(m[j], s[t & 1][0][j][3]), s[t & 1][1][j][0]);
      m[j] = fmaxf(fmaxf(m[j], s[t & 1][1][j][1]), s[t & 1][1][j][2]);
    }
    const float ma = fmaxf(fmaxf(m[0], m[1]), m[2]), mb = fmaxf(fmaxf(s[t & 1][1][0][3], s[t & 1][1][1][3]), s[t & 1][1][2][3]);
    const float mt = fmaxf(fmaxf(ma, mb), ma), mr = fmaxf(fmaxf(mrow[0], mrow[1]), mrow[2]);
    if (__any(!(fmaf(mt, CS, mr) <= THR))) {
      float nm[3];
#pragma unroll
      for (int j = 0; j < 3; ++j) nm[j] = fminf(mrow[j], -xmax16_32(lane_max(t & 1, j)) * CS);
      rescale(TI, nm);
      *(lds_f32*)(L + SCRF) = 1.f;
    }
  };
  f32x2 ea[2], eb[2], m2;
  auto exp_slot = [&](auto TI, auto UI) {
    constexpr int t = decltype(TI)::v, u = decltype(UI)::v;
    if constexpr (u >= 2 && u - 2 < 12) {
      constexpr int v = u - 2;
      lsum[v >> 2] += eb[v & 1];
      pr[t][v >> 2][v & 3] = __builtin_bit_cast(unsigned, __builtin_convertvector(eb[v & 1], bf16x2));
      asm volatile("" : "+v"(lsum[v >> 2]), "+v"(pr[t][v >> 2][v & 3]));
    }
    if constexpr (u >= 1 && u - 1 < 12) {
      constexpr int v = u - 1;
      eb[v & 1] = f32x2{__builtin_amdgcn_exp2f(ea[v & 1][0]), __builtin_amdgcn_exp2f(ea[v & 1][1])};
      asm volatile("" : "+v"(eb[v & 1]));
    }
    if constexpr (u < 12) {
      constexpr int j = u >> 2, kt = (u >> 1) & 1, r = 2 * (u & 1);
      if constexpr ((u & 3) == 0) { m2 = f32x2{mrow[j], mrow[j]}; asm volatile("" : "+v"(m2)); }
      ea[u & 1] = __builtin_elementwise_fma(f32x2{s[t & 1][kt][j][r], s[t & 1][kt][j][r + 1]}, f32x2{CS, CS}, m2);
      asm volatile("" : "+v"(ea[u & 1]));
    }
  };
  auto gap_slot = [](int gi) { return (gi * 7) % 12 < 7 ? (gi * 7) / 12 : -1; };
  {
    const unsigned krd = (32u * kq + i16) * 256u + ((g ^ i16) << 4);
    const unsigned qrd = QIMG + (48u * qg + i16) * 256u + ((g ^ i16) << 4);
    bf16x8 ka[2][2], qb[2][3];
    const unsigned qrd0 = opq(qrd);
    static_for<0, 4>([&](auto TI) {
      constexpr int t = decltype(TI)::v;
      constexpr unsigned slot = (unsigned)kvslot(t) * SLOT;
      if constexpr (t <= 1) bar_only(); else bar();
      auto rd = [&](auto KSI, auto GI) {
        constexpr int ks = decltype(KSI)::v, gap = decltype(GI)::v;
        if constexpr (ks < 4) {
          if constexpr (gap == 0) ka[ks & 1][0] = ld128(L, slot + (krd ^ (ks << 6)));
          if constexpr (gap >= 1 && gap <= 3 && !(ks == 0 && t > 0)) qb[ks & 1][gap - 1] = ld128(L, (qrd ^ (ks << 6)) + (gap - 1) * 4096u);
          if constexpr (gap == 4) ka[ks & 1][1] = ld128(L, slot + (krd ^ (ks << 6)) + 4096u);
        } else if constexpr (t < 3) {
          if constexpr (gap >= 1 && gap <= 3) qb[0][gap - 1] = ld128(L, qrd0 + (gap - 1) * 4096u);
        }
      };
      static_for<0, 5>([&](auto GI) { rd(IC<0>{}, GI); });
      if constexpr (t == 1) {
#pragma unroll
        for (int j = 0; j < 3; ++j) {
          float m = *(lds_f32*)(L + scr + j * 64u);
#pragma unroll
          for (int u = 1; u < 4; ++u) m = fmaxf(m, *(lds_f32*)(L + scr + u * 192u + j * 64u));
          mrow[j] = -m * CS;
        }
      }
      SB();
      static_for<0, 4>([&](auto KSI) {
        constexpr int ks = decltype(KSI)::v;
        auto nx = [&](auto GI) {
          constexpr int gap = decltype(GI)::v, sl = gap_slot(6 * ks + gap);
          rd(IC<ks + 1>{}, GI);
          if constexpr (t >= 1 && sl >= 0) exp_slot(IC<(t >= 1 ? t - 1 : 0)>{}, IC<(sl >= 0 ? sl : 0)>{});
        };
        STEP6(s[t & 1], ka[ks & 1], qb[ks & 1], (ks == 0), nx)
      });
      if constexpr (t == 0) {
#pragma unroll
        for (int j = 0; j < 3; ++j) {
          const float m = xmax16_32(lane_max(0, j));
          *(lds_f32*)(L + scw + j * 64u) = m;
        }
        if (w == 0) *(lds_f32*)(L + SCRF) = 0.f;
      } else check(TI);
    });
  }
  const unsigned pwr = opq(PB1 + w * 3072u + l * 16u), prd = qg * 12288u + l * 16u;
  auto p_out = [&](int t, int j) {
    *(lds_bf16x8*)(L + ((t & 1) ? 0u : PB0 - PB1) + pwr + j * 1024u) = __builtin_bit_cast(bf16x8, u32x4{pr[t][j][0], pr[t][j][1], pr[t][j][2], pr[t][j][3]});
  };
#pragma unroll
  for (int j = 0; j < 3; ++j) p_out(0, j);
  SB();
  f32x4 o[2][3];
  float inv[3];
  {
    const unsigned fV = 2u * (4u * (g & 1u) + q4);
    const unsigned vrd0 = (4u * g + q4) * 256u + (((4u * kq + (p4 >> 1)) ^ fV) << 4) + 8u * (p4 & 1u), vrd1 = vrd0 ^ 32u;
    bf16x8 va[2][2], pb[2][3], pl[4][3];
    static_for<0, 4>([&](auto TI) {
      constexpr int t = decltype(TI)::v;
      constexpr unsigned slot = (unsigned)kvslot(4 + t) * SLOT;
      const unsigned vb0 = opq(slot + vrd0), vb1 = opq(slot + vrd1), pbb = opq(((t & 1) ? PB1 : PB0) + prd);
      bar_only();
      auto rd = [&](auto KSI, auto GI) {
        constexpr int ks = decltype(KSI)::v, gap = decltype(GI)::v;
        if constexpr (ks < 4) {
          if constexpr (gap == 0) va[ks & 1][0] = ldtr2(L, vb0 + ks * 8192u, vb0 + ks * 8192u + 4096u);
          if constexpr (gap >= 1 && gap <= 3 && t < 3) pb[ks & 1][gap - 1] = ld128(L, pbb + ks * 3072u + (gap - 1) * 1024u);
          if constexpr (gap == 4) va[ks & 1][1] = ldtr2(L, vb1 + ks * 8192u, vb1 + ks * 8192u + 4096u);
        }
      };
      if constexpr (t == 0) {
        if (__builtin_amdgcn_readfirstlane(__float_as_uint(*(lds_f32*)(L + SCRF))) != 0u) {
#pragma unroll
          for (int j = 0; j < 3; ++j)
            *(lds_f32*)(L + scw + 2048u + j * 64u) = mrow[j];
          bar_only();
          float nm[3];
#pragma unroll
          for (int j = 0; j < 3; ++j) {
            float m = *(lds_f32*)(L + scr + 2048u + j * 64u);
#pragma unroll
            for (int u = 1; u < 4; ++u) m = fminf(m, *(lds_f32*)(L + scr + 2048u + u * 192u + j * 64u));
            nm[j] = m;
          }
          rescale(IC<3>{}, nm);
#pragma unroll
          for (int j = 0; j < 3; ++j) p_out(0, j);
          bar_only();
        }
      }
      static_for<0, 5>([&](auto GI) { rd(IC<0>{}, GI); });
      SB();
      static_for<0, 4>([&](auto KSI) {
        constexpr int ks = decltype(KSI)::v;
        auto nx = [&](auto GI) {
          constexpr int gap = decltype(GI)::v, sl = gap_slot(6 * ks + gap);
          rd(IC<ks + 1>{}, GI);
          if constexpr (t == 0 && sl >= 0) exp_slot(IC<3>{}, IC<(sl >= 0 ? sl : 0)>{});
          if constexpr (t < 3 && ks == 3 && gap < 3) p_out(t + 1, gap);
          if constexpr (t == 1 && ks < 3 && gap == 5) {
            const float sm = xsum16_32(lsum[ks][0] + lsum[ks][1]);
            *(lds_f32*)(L + SCRS + (w * 48u + 16u * ks + i16) * 4u) = sm;
          }
        };
        if constexpr (t < 3) { STEP6(o, va[ks & 1], pb[ks & 1], (t == 0 && ks == 0), nx) }
        else { STEP6(o, va[ks & 1], pl[ks], false, nx) }
      });
      if constexpr (t == 2) {
        const unsigned prd1 = opq(PB1 + prd);
        bar_only();
#pragma unroll
        for (int ks = 0; ks < 4; ++ks)
#pragma unroll
          for (int j = 0; j < 3; ++j) pl[ks][j] = ld128(L, prd1 + ks * 3072u + j * 1024u);
#pragma unroll
        for (int j = 0; j < 3; ++j) {
          float sum = 0.f;
#pragma unroll
          for (int u = 0; u < 4; ++u) sum += *(lds_f32*)(L + scr + (SCRS - SCRM) + u * 192u + j * 64u);
          inv[j] = __builtin_amdgcn_rcpf(sum);
        }
        SB();
      }
    });
  }
  const unsigned lt = fresh_lane(), gt = lt >> 4, it = lt & 15u;
  const srd_t srdWS = __builtin_amdgcn_make_buffer_rsrc((void*)ws, (short)0, (int)WS_END, 0x00020000);
#pragma unroll
  for (int j = 0; j < 3; ++j) {
    const bf16x8 pk = pack8(o[0][j] * inv[j], o[1][j] * inv[j]);
    st128_dev(pk, srdWS, lt * 16u, WS_O + bid * 24576u + ((3u * qg + j) * 4u + kq) * 1024u);
    *(lds_bf16x8*)(L + OIMG + ((3u * qg + j) * 4u + kq) * 1024u + lt * 16u) = pk;
  }
  const unsigned qh = w >> 2, nh = (w >> 1) & 1u, kp = w & 1u;
  f32x4 acc[5][3];
  unsigned wa[5], wb[5];
  {
    const unsigned wrow = 32u * kp + 4u * gt + (it >> 2);
#pragma unroll
    for (int nt = 0; nt < 5; ++nt) {
      const unsigned cc = nh * 10u + 2u * nt + ((lt & 3u) >> 1);
      wa[nt] = opq(wrow * 320u + ((cc ^ ((gt & 1u) << 1)) << 4) + 8u * (lt & 1u));
      wb[nt] = opq(wa[nt] + 81920u);
    }
  }
  bar_only();
  bf16x8 af[3];
  auto gemm = [&](auto S0I, auto S1I, auto BF) {
    constexpr int u0 = 5 * decltype(S0I)::v, u1 = 5 * decltype(S1I)::v;
    auto rda = [&](int u) {
      const int st = u / 5, nt = u % 5, ci = st >> 1;
      const unsigned cb = (unsigned)(ci == 0 ? 0 : 4 - ci) * 40960u + (unsigned)(st & 1) * 20480u;
      if (cb >= 81920u) af[u % 3] = ldtr2(L, cb - 81920u + wb[nt], cb - 81920u + wb[nt] + 5120u);
      else af[u % 3] = ldtr2(L, cb + wa[nt], cb + wa[nt] + 5120u);
    };
    rda(u0); rda(u0 + 1);
    static_for<u0, u1>([&](auto UI) {
      constexpr int u = decltype(UI)::v, st = u / 5, nt = u % 5;
      if constexpr (u + 2 < u1) rda(u + 2);
      SB();
#pragma unroll
      for (int j = 0; j < 3; ++j) acc[nt][j] = mfma16(af[u % 3], BF(st, j), st == 0 ? f32x4{0.f, 0.f, 0.f, 0.f} : acc[nt][j]);
      SB();
    });
  };
  {
    bf16x8 ob[2][3];
#pragma unroll
    for (int kk = 0; kk < 2; ++kk)
#pragma unroll
      for (int j = 0; j < 3; ++j) ob[kk][j] = ld128(L, OIMG + ((3u * qh + j) * 4u + kp + 2u * kk) * 1024u + lt * 16u);
    gemm(IC<0>{}, IC<2>{}, [&](int st, int j) { return ob[st & 1][j]; });
  }
  wait_vm_bar<0>();
  bar();
  {
    u32x4 of[3][2][3];
#pragma unroll
    for (int i = 0; i < 3; ++i) {
      const unsigned src = (bid & ~24u) | (((hh + 3u - (unsigned)i) & 3u) << 3);
#pragma unroll
      for (int kk = 0; kk < 2; ++kk)
#pragma unroll
        for (int j = 0; j < 3; ++j)
          of[i][kk][j] = __builtin_amdgcn_raw_buffer_load_b128(srdWS, lt * 16u, WS_O + src * 24576u + ((3u * qh + j) * 4u + kp + 2u * kk) * 1024u, 16);
    }
    SB();
    gemm(IC<2>{}, IC<8>{}, [&](int st, int j) { return __builtin_bit_cast(bf16x8, of[(st >> 1) - 1][st & 1][j]); });
  }
  {
    const unsigned xb = (w ^ 1u) * 4096u + lt * 16u, rb = w * 4096u + lt * 16u;
    const f32x4 z4 = {0.f, 0.f, 0.f, 0.f};
#define ACCF(f) ((f) < 15 ? acc[((f) % 15) / 3][(f) % 3] : z4)
    if (kp == 0) {
#pragma unroll
      for (int u = 4; u < 8; ++u) *(lds_bf16x8*)(L + xb + (u - 4) * 1024u) = pack8(ACCF(2 * u), ACCF(2 * u + 1));
    } else {
#pragma unroll
      for (int u = 0; u < 4; ++u) *(lds_bf16x8*)(L + xb + u * 1024u) = pack8(ACCF(2 * u), ACCF(2 * u + 1));
    }
    bf16x8 sel0, sel1;
#pragma unroll
    for (int jj = 0; jj < 4; ++jj) {
      const __bf16 vv = (4u * gt + jj == it) ? one : (__bf16)0.0f;
      sel0[jj] = vv; sel0[4 + jj] = (__bf16)0.0f; sel1[jj] = (__bf16)0.0f; sel1[4 + jj] = vv;
    }
    bar_only();
    bf16x8 rr[4];
#pragma unroll
    for (int u = 0; u < 4; ++u) rr[u] = ld128(L, rb + u * 1024u);
    const unsigned vo = WS_P + ((bid & ~7u) | (it >> 1)) * 32768u + xcd * 4096u + (gt * 2u + (lt & 1u)) * 16u;
    if (kp == 0) {
#pragma unroll
      for (int u = 0; u < 4; ++u)
        st128_dev(pack8(mfma16(sel0, rr[u], ACCF(2 * u)), mfma16(sel1, rr[u], ACCF(2 * u + 1))), srdWS, vo, ((w >> 1) * 8u + u) * 128u);
    } else {
#pragma unroll
      for (int u = 4; u < 8; ++u)
        st128_dev(pack8(mfma16(sel0, rr[u - 4], ACCF(2 * u)), mfma16(sel1, rr[u - 4], ACCF(2 * u + 1))), srdWS, vo, ((w >> 1) * 8u + u) * 128u);
    }
  }
  const unsigned c = w * 32u + (lt & 31u), hf = lt >> 5;
  __bf16* optr;
  {
    const unsigned pw = c >> 6, f = 2u * ((c >> 3) & 7u) + hf, sl = 16u * ((c & 7u) >> 1) + 2u * xcd + (c & 1u);
    const unsigned q = 48u * (pw >> 1) + 16u * (f % 3u) + (sl & 15u), n = 160u * hh + 80u * (pw & 1u) + 16u * (f / 3u) + 4u * (sl >> 4);
    optr = f < 15u ? out + (size_t)(b * SQ + q) * DM + n : nullptr;
    asm volatile("" : "+v"(optr));
  }
  wait_vm_bar<0>();
  bar();
  {
    u32x4 r[4];
#pragma unroll
    for (int i = 0; i < 4; ++i) r[i] = __builtin_amdgcn_raw_buffer_load_b128(srdWS, c * 16u + hf * 16384u, WS_P + bid * 32768u + i * 4096u, 16);
    f32x4 f0 = {0.f, 0.f, 0.f, 0.f}, f1 = f0;
#pragma unroll
    for (int i = 0; i < 4; ++i) {
      const bf16x8 v = __builtin_bit_cast(bf16x8, r[i]);
#pragma unroll
      for (int q = 0; q < 4; ++q) { f0[q] += (float)v[q]; f1[q] += (float)v[4 + q]; }
    }
    f32x4 fs;
#pragma unroll
    for (int q = 0; q < 4; ++q) {
      auto sw = __builtin_amdgcn_permlane32_swap(__float_as_uint(f0[q]), __float_as_uint(f1[q]), false, false);
      fs[q] = __uint_as_float(sw[0]) + __uint_as_float(sw[1]);
    }
    if (optr) *(bf16x4*)optr = cvt4(fs);
  }
}

__global__ __launch_bounds__(768, 3)
void gqa_fused(const __bf16* __restrict__ x, const __bf16* __restrict__ kc, const __bf16* __restrict__ vc,
               const __bf16* __restrict__ wq, const __bf16* __restrict__ wo, char* __restrict__ ws, __bf16* __restrict__ out) {
  extern __shared__ __attribute__((aligned(16))) char smem[];
  lds_char* L = (lds_char*)smem;
  const unsigned ldsb = (unsigned)(size_t)L;
  const unsigned tid = threadIdx.x, l = tid & 63u;
  unsigned w = __builtin_amdgcn_readfirstlane(tid >> 6);
  asm volatile("s_nop 4" : "+s"(w));
  const unsigned bid = blockIdx.x;
  unsigned long long pc; unsigned len;
  asm volatile("s_getpc_b64 %0\n.Lgqa_code_begin:\n\ts_mov_b32 %1, .Lgqa_code_end - .Lgqa_code_begin" : "=s"(pc), "=s"(len));
  if (__builtin_expect(w < 4u, 1)) loader(L, ldsb, l, w, bid, x, kc, vc, wq, wo, ws);
  else {
    unsigned cpf;
    w -= 4u;
    unsigned off = ((bid >> 3) * 8u + w) * 256u + l * 4u;
    off = off < len ? off : 0u;
    asm volatile("global_load_dword %0, %1, %2" : "=&v"(cpf) : "v"(off), "s"(pc) : "memory");
    compute(L, l, w, bid, wo, ws, out, cpf);
    asm volatile(".Lgqa_code_end:");
  }
}

extern "C" void kernel_launch(void* const* d_in, const int* in_sizes, int n_in, void* d_out, int out_size, void* d_ws,
                              size_t ws_size, hipStream_t stream) {
  (void)hipFuncSetAttribute((const void*)gqa_fused, hipFuncAttributeMaxDynamicSharedMemorySize, LDS_BYTES);
  hipLaunchKernelGGL(gqa_fused, dim3(256), dim3(768), LDS_BYTES, stream, (const __bf16*)d_in[0], (const __bf16*)d_in[1],
                     (const __bf16*)d_in[2], (const __bf16*)d_in[3], (const __bf16*)d_in[4], (char*)d_ws, (__bf16*)d_out);
}
```
